```python
import torch

def _softmax(x, axis=-1):
    return torch.softmax(x, dim=axis)
def _gelu(x):
    return torch.nn.functional.gelu(x, approximate="tanh")
def _silu(x):
    return torch.nn.functional.silu(x)
def _sigmoid(x):
    return torch.sigmoid(x)
def _layernorm(x, axis=-1, eps=1e-5):
    m = x.mean(dim=axis, keepdim=True)
    v = x.var(dim=axis, keepdim=True, unbiased=False)
    return (x - m) / torch.sqrt(v + eps)
def _scatter_add(tgt, idx, src, axis=0):
    return tgt.clone().index_add_(axis, idx, src)
def _segment_sum(src, seg, n):
    out = torch.zeros((n,) + src.shape[1:], dtype=src.dtype)
    return out.index_add_(0, seg, src)
def _pad(x, pw):
    flat = tuple(v for pair in reversed(pw) for v in pair)
    return torch.nn.functional.pad(x, flat)

kernel_name = 'interleave_layers__softmax_full_mha__glu_gated_reglu__prompt'

def setup_inputs():

    _w = (lambda a: (lambda t: t.to(torch.bfloat16) if t.is_floating_point() else t)(torch.as_tensor(a)))
    def _A_setup():
        B, S, D = 1, 2048, 640
        return {
            "x": _w((lambda *s: torch.randn(*s, dtype=torch.bfloat16) * 0.05)(B, S, D)),
            "wqkv": _w((lambda *s: torch.randn(*s, dtype=torch.bfloat16) * 0.05)(D, 3 * D)),
            "wo": _w((lambda *s: torch.randn(*s, dtype=torch.bfloat16) * 0.05)(D, D)),
        }
    _A_in = _A_setup()

    def _B_setup():
        B, S, D, F = 1, 2048, 640, 1536
        return {
            "x": _w((lambda *s: torch.randn(*s, dtype=torch.bfloat16) * 0.05)(B, S, D)),
            "W_gate": _w((lambda *s: torch.randn(*s, dtype=torch.bfloat16) * 0.8)(D, F) * (2.0 / D) ** 0.5),
            "W_up": _w((lambda *s: torch.randn(*s, dtype=torch.bfloat16) * 0.8)(D, F) * (2.0 / D) ** 0.5),
            "W_down": _w((lambda *s: torch.randn(*s, dtype=torch.bfloat16) * 0.8)(F, D) * (1.0 / F) ** 0.5),
        }
    _B_in = _B_setup()

    return {
        "A_x": _A_in["x"],
        "A_wqkv": _A_in["wqkv"],
        "A_wo": _A_in["wo"],
        "B_x": _B_in["x"],
        "B_W_gate": _B_in["W_gate"],
        "B_W_up": _B_in["W_up"],
        "B_W_down": _B_in["W_down"],
    }

def reference(A_x, A_wqkv, A_wo, B_x, B_W_gate, B_W_up, B_W_down):
    def _A_ref(x, wqkv, wo):
        B, S, D = x.shape
        H = 32; Dh = D // H
        qkv = (lambda x, *d: x.permute(*d))((x @ wqkv).reshape(B, S, 3, H, Dh), 2, 0, 3, 1, 4)
        q, k, v = qkv[0], qkv[1], qkv[2]
        scale = Dh ** -0.5
        scores = torch.einsum("bhqd,bhkd->bhqk", q, k) * scale
        p = _softmax(scores, axis=-1)
        o = torch.einsum("bhqk,bhkd->bhqd", p, v)
        return (lambda x, *d: x.permute(*d))(o, 0, 2, 1, 3).reshape(B, S, D) @ wo

    def _B_ref(x, W_gate, W_up, W_down):
        gate = torch.einsum("bsd,df->bsf", x, W_gate)
        up = torch.einsum("bsd,df->bsf", x, W_up)
        gate = gate * (lambda x, dt: x.to(dt))(gate > 0, gate.dtype)
        h = gate * up
        return torch.einsum("bsf,fd->bsd", h, W_down)

    x = A_x
    x = x + _A_ref(_layernorm(x, axis=-1), A_wqkv, A_wo)
    x = x + _B_ref(_layernorm(x, axis=-1), B_W_gate, B_W_up, B_W_down)
    x = x + _A_ref(_layernorm(x, axis=-1), A_wqkv, A_wo)
    x = x + _B_ref(_layernorm(x, axis=-1), B_W_gate, B_W_up, B_W_down)
    return x
```

```cpp
#include <hip/hip_runtime.h>
#include <hip/hip_bf16.h>
#include <stdint.h>

typedef __attribute__((ext_vector_type(8))) __bf16 bf16x8;
typedef __attribute__((ext_vector_type(16))) __bf16 bf16x16;
typedef __attribute__((ext_vector_type(4))) short s16x4;
typedef __attribute__((ext_vector_type(2))) float f32x2;
typedef __attribute__((ext_vector_type(4))) float f32x4;
typedef __attribute__((ext_vector_type(8))) float f32x8;
typedef __attribute__((ext_vector_type(16))) float f32x16;
typedef __attribute__((ext_vector_type(32))) float f32x32;
typedef __attribute__((ext_vector_type(2))) unsigned u32x2;
typedef __attribute__((ext_vector_type(4))) unsigned u32x4;
#define LAS __attribute__((address_space(3)))
#define GAS __attribute__((address_space(1)))

constexpr int SEQ = 2048, DM = 640, NH = 32, FF = 1536;
constexpr float LN_EPS = 1e-5f;
constexpr float QSCALE = 0.22360679774997896f * 1.4426950408889634f;

constexpr size_t KVPAIR = 5376, KVCHUNK = 4 * KVPAIR;
constexpr size_t WS_WQKV = 0;
constexpr size_t WS_WO   = WS_WQKV + 32 * 2 * 40 * 1024;
constexpr size_t WS_WGU  = WS_WO + 8 * 20 * 5 * 1024;
constexpr size_t WS_WD   = WS_WGU + 16 * 40 * 6 * 1024;
constexpr size_t WS_CSQ  = WS_WD + 8 * 48 * 5 * 1024;
constexpr size_t WS_CSG  = WS_CSQ + 2048 * 4;
constexpr size_t WS_ST   = WS_CSG + 3072 * 4;
constexpr size_t ST_BYTES = 2048 * 8 * 8;
constexpr size_t WS_X    = WS_ST + 2 * ST_BYTES;
constexpr size_t X_BYTES = 64 * 40 * 1024;
constexpr size_t WS_KV   = WS_X + 2 * X_BYTES;
constexpr size_t WS_Q    = WS_KV + 32 * 8 * KVCHUNK;
constexpr size_t WS_OM   = WS_Q + 64 * 32 * 1536;
constexpr size_t WS_OT   = WS_OM + 64 * 32 * 1024;
constexpr size_t WS_H    = WS_OT + 64 * 32 * 256;
constexpr size_t WS_END  = WS_H + 64 * 96 * 1024;

__device__ __forceinline__ unsigned cvtpk(float lo, float hi) {
    typedef __attribute__((ext_vector_type(2))) __bf16 bf16x2;
    bf16x2 r = {(__bf16)lo, (__bf16)hi};
    return __builtin_bit_cast(unsigned, r);
}
__device__ __forceinline__ float bflo(unsigned u) { return __uint_as_float(u << 16); }
__device__ __forceinline__ float bfhi(unsigned u) { return __uint_as_float(u & 0xffff0000u); }
__device__ __forceinline__ float swap32_add(float v) {
    auto r = __builtin_amdgcn_permlane32_swap(__float_as_uint(v), __float_as_uint(v), false, false);
    return __uint_as_float(r[0]) + __uint_as_float(r[1]);
}
__device__ __forceinline__ float swap16_add(float v) {
    auto r = __builtin_amdgcn_permlane16_swap(__float_as_uint(v), __float_as_uint(v), false, false);
    return __uint_as_float(r[0]) + __uint_as_float(r[1]);
}
__device__ __forceinline__ int phi(int s, int h, int j) { return 16 * s + 8 * (j >> 2) + 4 * h + (j & 3); }

#define MFMA32(a, b, c) __builtin_amdgcn_mfma_f32_32x32x16_bf16(__builtin_bit_cast(bf16x8, a), __builtin_bit_cast(bf16x8, b), c, 0, 0, 0)
#define MFMA16(a, b, c) __builtin_amdgcn_mfma_f32_16x16x32_bf16(__builtin_bit_cast(bf16x8, a), __builtin_bit_cast(bf16x8, b), c, 0, 0, 0)
#define SBAR() __builtin_amdgcn_sched_barrier(0)

template <int OFF> __device__ __forceinline__ void gload16(u32x4& r, const char* sbase, unsigned voff) {
    asm volatile("global_load_dwordx4 %0, %1, %2 offset:%3" : "=v"(r) : "v"(voff), "s"(sbase), "n"(OFF) : "memory");
}
template <int OFF> __device__ __forceinline__ void gload8(u32x2& r, const char* sbase, unsigned voff) {
    asm volatile("global_load_dwordx2 %0, %1, %2 offset:%3" : "=v"(r) : "v"(voff), "s"(sbase), "n"(OFF) : "memory");
}
template <int N> __device__ __forceinline__ void glds(const char* sbase, unsigned voff, unsigned lds) {
    if constexpr (N == 1) asm volatile("s_mov_b32 m0, %2\n\ts_nop 0\n\tglobal_load_lds_dwordx4 %0, %1" :: "v"(voff), "s"(sbase), "s"(lds) : "memory");
    if constexpr (N == 2) asm volatile("s_mov_b32 m0, %2\n\ts_nop 0\n\tglobal_load_lds_dwordx4 %0, %1\n\tglobal_load_lds_dwordx4 %0, %1 offset:1024" :: "v"(voff), "s"(sbase), "s"(lds) : "memory");
    if constexpr (N == 3) asm volatile("s_mov_b32 m0, %2\n\ts_nop 0\n\tglobal_load_lds_dwordx4 %0, %1\n\tglobal_load_lds_dwordx4 %0, %1 offset:1024\n\tglobal_load_lds_dwordx4 %0, %1 offset:2048" :: "v"(voff), "s"(sbase), "s"(lds) : "memory");
    if constexpr (N == 4) asm volatile("s_mov_b32 m0, %2\n\ts_nop 0\n\tglobal_load_lds_dwordx4 %0, %1\n\tglobal_load_lds_dwordx4 %0, %1 offset:1024\n\tglobal_load_lds_dwordx4 %0, %1 offset:2048\n\tglobal_load_lds_dwordx4 %0, %1 offset:3072" :: "v"(voff), "s"(sbase), "s"(lds) : "memory");
}
template <int N> __device__ __forceinline__ void wait_bar() { asm volatile("s_waitcnt vmcnt(%0) lgkmcnt(0)\n\ts_barrier" :: "n"(N) : "memory"); }
template <int N> __device__ __forceinline__ void wait_vm() { asm volatile("s_waitcnt vmcnt(%0)" :: "n"(N) : "memory"); }
#define PIN(x) asm volatile("" : "+v"(x))
template <int I, int N, class F> __device__ __forceinline__ void sfor(F&& f) {
    if constexpr (I < N) { f(std::integral_constant<int, I>{}); sfor<I + 1, N>(f); }
}
__device__ __forceinline__ u32x4 lds16(unsigned a) { return *(const LAS u32x4*)a; }
__device__ __forceinline__ u32x2 lds8(unsigned a) { return *(const LAS u32x2*)a; }

__device__ __forceinline__ void ln_stats(const u32x4& p0, const u32x4& p1, float& mean, float& rstd) {
    const float s0 = __uint_as_float(p0[0]), s1 = __uint_as_float(p0[2]), s2 = __uint_as_float(p1[0]), s3 = __uint_as_float(p1[2]);
    mean = swap32_add((s0 + s1) + (s2 + s3)) * (1.f / DM);
    const float d0 = s0 * (1.f / 80) - mean, d1 = s1 * (1.f / 80) - mean, d2 = s2 * (1.f / 80) - mean, d3 = s3 * (1.f / 80) - mean;
    float m2 = (__uint_as_float(p0[1]) + __uint_as_float(p0[3])) + (__uint_as_float(p1[1]) + __uint_as_float(p1[3]));
    m2 += 80.f * ((d0 * d0 + d1 * d1) + (d2 * d2 + d3 * d3));
    m2 = swap32_add(m2);
    rstd = 1.f / sqrtf(m2 * (1.f / DM) + LN_EPS);
}

__device__ __forceinline__ void transpose8x4(const u32x2 (&r)[8], u32x4 (&o)[4]) {
#pragma unroll
    for (int d = 0; d < 4; ++d) {
        o[0][d] = __builtin_amdgcn_perm(r[2 * d + 1][0], r[2 * d][0], 0x05040100u);
        o[1][d] = __builtin_amdgcn_perm(r[2 * d + 1][0], r[2 * d][0], 0x07060302u);
        o[2][d] = __builtin_amdgcn_perm(r[2 * d + 1][1], r[2 * d][1], 0x05040100u);
        o[3][d] = __builtin_amdgcn_perm(r[2 * d + 1][1], r[2 * d][1], 0x07060302u);
    }
}
__device__ __forceinline__ void store4(char* p, const u32x4 (&o)[4]) {
#pragma unroll
    for (int i = 0; i < 4; ++i) *(u32x4*)(p + 16 * i) = o[i];
}
__device__ __forceinline__ void qkv_slot(int which, int dg, int& tile, int& G) {
    if (dg < 4) { tile = which == 0 ? 0 : 1; G = which == 2 ? 4 + dg : dg; }
    else { tile = 0; G = which == 0 ? 4 : 5 + which; }
}
constexpr int PB_QKV = 150, PB_GU = 240, PB_O = 50, PB_D = 120, PB_X = 128, PB_CS = 78;
constexpr int PREP_BLOCKS = PB_QKV + PB_GU + PB_O + PB_D + PB_X + PB_CS;

__global__ __launch_bounds__(256) void k_prep(const uint16_t* __restrict__ x, const uint16_t* __restrict__ wqkv, const uint16_t* __restrict__ wo,
                                              const uint16_t* __restrict__ wg, const uint16_t* __restrict__ wu, const uint16_t* __restrict__ wd, char* __restrict__ ws) {
    __shared__ float red[32][64];
    int b = blockIdx.x; const int tid = threadIdx.x;
    u32x2 r[8]; u32x4 o[4];
    if (b < PB_X) {
        const int wv = b * 4 + (tid >> 6), T = wv >> 3, cb = wv & 7, lane = tid & 63, t = lane & 31, h = lane >> 5;
        const uint16_t* src = x + (size_t)(32 * T + t) * DM + 80 * cb + 8 * h;
        u32x4 v[5];
#pragma unroll
        for (int i = 0; i < 5; ++i) v[i] = *(const u32x4*)(src + 16 * i);
        float sum = 0.f;
#pragma unroll
        for (int i = 0; i < 5; ++i)
#pragma unroll
            for (int d = 0; d < 4; ++d) sum += bflo(v[i][d]) + bfhi(v[i][d]);
        sum = swap32_add(sum);
        const float m = sum * (1.f / 80); float m2 = 0.f;
#pragma unroll
        for (int i = 0; i < 5; ++i)
#pragma unroll
            for (int d = 0; d < 4; ++d) { const float a = bflo(v[i][d]) - m, c = bfhi(v[i][d]) - m; m2 += a * a + c * c; }
        m2 = swap32_add(m2);
        if (h == 0) *(f32x2*)(ws + WS_ST + ((size_t)(32 * T + t) * 8 + cb) * 8) = (f32x2){sum, m2};
#pragma unroll
        for (int i = 0; i < 5; ++i) {
            auto s0 = __builtin_amdgcn_permlane32_swap(v[i][0], v[i][2], false, false);
            auto s1 = __builtin_amdgcn_permlane32_swap(v[i][1], v[i][3], false, false);
            *(u32x4*)(ws + WS_X + ((size_t)(T * 40 + 5 * cb + i) * 64 + lane) * 16) = (u32x4){s0[0], s1[0], s0[1], s1[1]};
        }
        return;
    }
    b -= PB_X;
    if (b < PB_QKV) {
        const int g = b * 256 + tid, cg = g % 480, sh = g / 480, s = sh >> 1, h = sh & 1;
#pragma unroll
        for (int j = 0; j < 8; ++j) r[j] = *(const u32x2*)(wqkv + (size_t)phi(s, h, j) * (3 * DM) + 4 * cg);
        transpose8x4(r, o);
        const int which = cg / 160, c = (cg % 160) / 5, dg = cg % 5; int tile, G; qkv_slot(which, dg, tile, G);
        char* p = ws + WS_WQKV + ((size_t)((c * 2 + tile) * 40 + s) * 64 + h * 32 + 4 * G) * 16;
        store4(p, o);
        if (which == 0 && dg == 4) store4(p + 64, o);
        return;
    }
    b -= PB_QKV;
    if (b < PB_GU) {
        const int g = b * 256 + tid, fg = g % 384, rest = g / 384, up = rest & 1, h = (rest >> 1) & 1, s = rest >> 2;
        const uint16_t* w = up ? wu : wg;
#pragma unroll
        for (int j = 0; j < 8; ++j) r[j] = *(const u32x2*)(w + (size_t)phi(s, h, j) * FF + 4 * fg);
        transpose8x4(r, o);
        const int sg = fg >> 2, gi = fg & 3, G = (gi & 1) + 4 * (gi >> 1) + 2 * up, cb = sg / 6, n = sg % 6;
        store4(ws + WS_WGU + ((size_t)((cb * 40 + s) * 6 + n) * 64 + h * 32 + 4 * G) * 16, o);
        return;
    }
    b -= PB_GU;
    if (b < PB_O + PB_D) {
        const bool isO = b < PB_O; if (!isO) b -= PB_O;
        const int g = b * 256 + tid, cg = g % 160, rest = g / 160, q = rest & 3, S = rest >> 2;
        const uint16_t* w = isO ? wo : wd;
#pragma unroll
        for (int j = 0; j < 8; ++j) {
            int k;
            if (!isO) k = phi(2 * S + (q >> 1), q & 1, j);
            else if (S < 16) k = 20 * (2 * S + (q >> 1)) + phi(0, q & 1, j);
            else k = 20 * (8 * (S - 16) + 2 * q + (j >> 2)) + 16 + (j & 3);
            r[j] = *(const u32x2*)(w + (size_t)k * DM + 4 * cg);
        }
        transpose8x4(r, o);
        const int cb = cg / 20, rr = cg % 20, sl = rr >> 2, gi = rr & 3;
        const int n = sl < 4 ? 2 * (sl >> 1) + (gi >> 1) : 4, qq = sl < 4 ? 2 * (sl & 1) + (gi & 1) : gi;
        store4(ws + (isO ? WS_WO : WS_WD) + ((size_t)((cb * (isO ? 20 : 48) + S) * 5 + n) * 64 + q * 16 + 4 * qq) * 16, o);
        return;
    }
    b -= PB_O + PB_D;
    {
        const int mat = b < 30 ? 0 : (b < 54 ? 1 : 2), c0 = 64 * (mat == 0 ? b : (mat == 1 ? b - 30 : b - 54));
        const uint16_t* w = mat == 0 ? wqkv : (mat == 1 ? wg : wu); const int ld = mat == 0 ? 3 * DM : FF;
        const int rg = tid >> 3, c8 = tid & 7;
        float a[8] = {0, 0, 0, 0, 0, 0, 0, 0};
        u32x4 v[20];
#pragma unroll
        for (int i = 0; i < 20; ++i) v[i] = *(const u32x4*)(w + (size_t)(rg + 32 * i) * ld + c0 + 8 * c8);
#pragma unroll
        for (int i = 0; i < 20; ++i)
#pragma unroll
            for (int d = 0; d < 4; ++d) { a[2 * d] += bflo(v[i][d]); a[2 * d + 1] += bfhi(v[i][d]); }
#pragma unroll
        for (int e = 0; e < 8; ++e) red[rg][8 * c8 + e] = a[e];
        __syncthreads();
        if (tid < 64) {
            float sum = 0.f;
#pragma unroll
            for (int i = 0; i < 32; ++i) sum += red[i][tid];
            const int col = c0 + tid, cg = col >> 2, i = col & 3;
            if (mat == 0) {
                const int which = cg / 160, c = (cg % 160) / 5, dg = cg % 5; int tile, G; qkv_slot(which, dg, tile, G);
                float* p = (float*)(ws + WS_CSQ) + (c * 2 + tile) * 32 + 4 * G + i;
                p[0] = sum; if (which == 0 && dg == 4) p[4] = sum;
            } else {
                const int sg = cg >> 2, gi = cg & 3, G = (gi & 1) + 4 * (gi >> 1) + 2 * (mat - 1);
                ((float*)(ws + WS_CSG))[sg * 32 + 4 * G + i] = sum;
            }
        }
    }
}

constexpr int QKV_LDS = 80 * 1024 + (int)KVCHUNK;
__global__ __launch_bounds__(512, 2) void k_qkv(char* __restrict__ ws, int xb) {
    extern __shared__ __attribute__((aligned(16))) char smem[];
    const int tid = threadIdx.x, lane = tid & 63, w = __builtin_amdgcn_readfirstlane(tid >> 6), t = lane & 31, h = lane >> 5;
    const int g = blockIdx.x & 7, c = blockIdx.x >> 3, T = g * 8 + w;
    const unsigned lds0 = (unsigned)(uintptr_t)smem, voff = lane * 16;
    {
        const char* wsrc = ws + WS_WQKV + (size_t)c * 80 * 1024 + w * 5 * 1024;
        glds<4>(wsrc, voff, lds0 + w * 5120); glds<1>(wsrc + 4096, voff, lds0 + w * 5120 + 4096);
        glds<4>(wsrc + 40960, voff, lds0 + 40960 + w * 5120); glds<1>(wsrc + 40960 + 4096, voff, lds0 + 40960 + w * 5120 + 4096);
    }
    u32x4 a[40], st0, st1, cs[8];
    const char* xsrc = ws + WS_X + (size_t)xb * X_BYTES + (size_t)T * 40 * 1024;
    sfor<0, 40>([&](auto I) { constexpr int i = I; gload16<(i & 3) * 1024>(a[i], xsrc + (i >> 2) * 4096, voff); });
    const char* ssrc = ws + WS_ST + (size_t)xb * ST_BYTES + (size_t)T * 32 * 64;
    gload16<0>(st0, ssrc, t * 64 + h * 32); gload16<16>(st1, ssrc, t * 64 + h * 32);
    const char* csrc = ws + WS_CSQ + c * 256;
    sfor<0, 8>([&](auto I) { constexpr int i = I; gload16<i * 32>(cs[i], csrc, h * 16); });
    wait_bar<50>();
    f32x16 acc0 = {}, acc1 = {};
    const unsigned wa = lds0 + voff;
    sfor<0, 10>([&](auto C) { constexpr int ch = C;
        asm volatile("s_waitcnt vmcnt(%4)" : "+v"(a[4 * ch]), "+v"(a[4 * ch + 1]), "+v"(a[4 * ch + 2]), "+v"(a[4 * ch + 3]) : "n"(46 - 4 * ch));
        sfor<0, 4>([&](auto I) { constexpr int s = 4 * ch + I;
            acc0 = MFMA32(lds16(wa + s * 1024), a[s], acc0);
            acc1 = MFMA32(lds16(wa + 40960 + s * 1024), a[s], acc1);
        });
    });
    asm volatile("s_waitcnt vmcnt(0)" : "+v"(st0), "+v"(st1), "+v"(cs[0]), "+v"(cs[1]), "+v"(cs[2]), "+v"(cs[3]), "+v"(cs[4]), "+v"(cs[5]), "+v"(cs[6]), "+v"(cs[7]));
    float mean, rstd; ln_stats(st0, st1, mean, rstd);
    float v0[16], v1[16];
#pragma unroll
    for (int r = 0; r < 16; ++r) {
        v0[r] = (acc0[r] - mean * __uint_as_float(cs[r >> 2][r & 3])) * rstd;
        v1[r] = (acc1[r] - mean * __uint_as_float(cs[4 + (r >> 2)][r & 3])) * rstd;
    }
    {
        char* qd = ws + WS_Q + (size_t)(T * 32 + c) * 1536;
        *(u32x4*)(qd + lane * 16) = (u32x4){cvtpk(v0[0] * QSCALE, v0[1] * QSCALE), cvtpk(v0[2] * QSCALE, v0[3] * QSCALE), cvtpk(v0[4] * QSCALE, v0[5] * QSCALE), cvtpk(v0[6] * QSCALE, v0[7] * QSCALE)};
        *(u32x2*)(qd + 1024 + lane * 8) = (u32x2){cvtpk(v0[8] * QSCALE, v0[9] * QSCALE), cvtpk(v0[10] * QSCALE, v0[11] * QSCALE)};
    }
    {
        const unsigned img = lds0 + 80 * 1024, pb = img + (w >> 1) * (unsigned)KVPAIR; const int b = w & 1;
        *(LAS u32x4*)(pb + b * 1024 + lane * 16) = (u32x4){cvtpk(v1[0], v1[1]), cvtpk(v1[2], v1[3]), cvtpk(v1[4], v1[5]), cvtpk(v1[6], v1[7])};
        const unsigned x0 = cvtpk(v0[12], v0[13]), x1 = cvtpk(v0[14], v0[15]);
        const unsigned vb = pb + 2560 + (2 * b + (t >> 4)) * 704 + ((t >> 3) & 1) * 352 + (t & 7) * 2;
#pragma unroll
        for (int i = 0; i < 8; i += 2) {
            const unsigned p = cvtpk(v1[8 + i], v1[9 + i]); const int d = 8 * (i >> 2) + (i & 3);
            *(LAS uint16_t*)(vb + (d + 4 * h) * 16) = (uint16_t)p;
            *(LAS uint16_t*)(vb + (d + 1 + 4 * h) * 16) = (uint16_t)(p >> 16);
        }
        if (h == 0) *(LAS u32x2*)(pb + 2048 + b * 256 + t * 8) = (u32x2){x0, x1};
        else {
            *(LAS uint16_t*)(vb + 16 * 16) = (uint16_t)x0; *(LAS uint16_t*)(vb + 17 * 16) = (uint16_t)(x0 >> 16);
            *(LAS uint16_t*)(vb + 18 * 16) = (uint16_t)x1; *(LAS uint16_t*)(vb + 19 * 16) = (uint16_t)(x1 >> 16);
        }
        if (tid < 64) {
            const int st = tid >> 2; const unsigned one = (tid & 1) ? 0u : 0x3f803f80u;
            *(LAS u32x4*)(img + (st >> 2) * (unsigned)KVPAIR + 2560 + (st & 3) * 704 + ((tid >> 1) & 1) * 352 + (20 + (tid & 1)) * 16) = (u32x4){one, one, one, one};
        }
        __syncthreads();
        char* kvd = ws + WS_KV + (size_t)(c * 8 + g) * KVCHUNK;
#pragma unroll
        for (int i = 0; i < 3; ++i) { const int o = (i * 512 + tid) * 16; if (o < (int)KVCHUNK) *(u32x4*)(kvd + o) = lds16(img + o); }
    }
}

constexpr int ATT_LDS = 4 * (int)KVCHUNK + 512;
#define M2B(kt, qt, c) __builtin_amdgcn_mfma_f32_32x32x4bf16_1k(__builtin_bit_cast(s16x4, kt), __builtin_bit_cast(s16x4, qt), c, 0, 0, 0)
template <int O> __device__ __forceinline__ f32x16 half16(const f32x32& s) {
    return __builtin_shufflevector(s, s, O, O + 1, O + 2, O + 3, O + 4, O + 5, O + 6, O + 7, O + 8, O + 9, O + 10, O + 11, O + 12, O + 13, O + 14, O + 15);
}
template <int O> __device__ __forceinline__ f32x8 oct(const f32x16& s) { return __builtin_shufflevector(s, s, O, O + 1, O + 2, O + 3, O + 4, O + 5, O + 6, O + 7); }
#define PVMAC(o, v, p8, idx) __builtin_amdgcn_smfmac_f32_32x32x32_bf16(__builtin_bit_cast(bf16x8, v), __builtin_bit_cast(bf16x16, p8), o, idx, 0, 0)
#define EXP4(S, i) do { S[i] = __builtin_amdgcn_exp2f(S[i]); S[i + 1] = __builtin_amdgcn_exp2f(S[i + 1]); S[i + 2] = __builtin_amdgcn_exp2f(S[i + 2]); S[i + 3] = __builtin_amdgcn_exp2f(S[i + 3]); PIN(S); SBAR(); } while (0)

__global__ __launch_bounds__(512, 2) void k_attn(char* __restrict__ ws) {
    extern __shared__ __attribute__((aligned(16))) char smem[];
    const int tid = threadIdx.x, lane = tid & 63, w = __builtin_amdgcn_readfirstlane(tid >> 6), t = lane & 31, h = lane >> 5;
    const int g = blockIdx.x & 7, c = blockIdx.x >> 3, T = g * 8 + w;
    const unsigned lds0 = (unsigned)(uintptr_t)smem, voff = lane * 16;
    const char* kv = ws + WS_KV + (size_t)c * 8 * KVCHUNK;
    const int pc = w < 7 ? 3 * w : 18;
    auto dma = [&](int j) { glds<3>(kv + (size_t)j * KVCHUNK + pc * 1024, voff, lds0 + (j & 3) * (unsigned)KVCHUNK + pc * 1024); };
    u32x4 qm; u32x2 qt;
    const char* qs = ws + WS_Q + (size_t)(T * 32 + c) * 1536;
    dma(0);
    gload16<0>(qm, qs, voff); gload8<1024>(qt, qs, lane * 8);
    dma(1); dma(2);
    asm volatile("s_waitcnt vmcnt(6)\n\ts_barrier" : "+v"(qm), "+v"(qt) :: "memory");
    const int idx = (int)0xDDDDDDDDu;
    const unsigned kb0 = lds0 + lane * 16, tb0 = lds0 + 2048 + lane * 8, vb0 = lds0 + 2560 + h * 352 + t * 16;
    unsigned kb = kb0, tb = tb0, vb = vb0;
    f32x16 A0, A1, B0, B1, o = {};
    u32x4 km0 = lds16(kb), km1 = lds16(kb + 1024); u32x2 kt = lds8(tb);
    { f32x32 s = M2B(kt, qt, (f32x32){}); A0 = MFMA32(km0, qm, half16<0>(s)); A1 = MFMA32(km1, qm, half16<16>(s)); }
    km0 = lds16(kb + KVPAIR); km1 = lds16(kb + KVPAIR + 1024); kt = lds8(tb + KVPAIR);
    u32x4 v0 = lds16(vb), v1 = lds16(vb + 704), v2 = lds16(vb + 1408), v3 = lds16(vb + 2112);
#define STEP(C0, C1, N0, N1, KO, VO) do { SBAR(); \
        f32x32 s_ = M2B(kt, qt, (f32x32){}); SBAR(); kt = lds8(tb + (KO)); SBAR(); \
        EXP4(C0, 0); EXP4(C0, 4); EXP4(C0, 8); \
        o = PVMAC(o, v0, oct<0>(C0), idx); SBAR(); v0 = lds16(vb + (VO)); SBAR(); \
        EXP4(C0, 12); EXP4(C1, 0); \
        N0 = MFMA32(km0, qm, half16<0>(s_)); SBAR(); km0 = lds16(kb + (KO)); SBAR(); \
        EXP4(C1, 4); \
        o = PVMAC(o, v1, oct<8>(C0), idx); SBAR(); v1 = lds16(vb + (VO) + 704); SBAR(); \
        C1[8] = __builtin_amdgcn_exp2f(C1[8]); C1[9] = __builtin_amdgcn_exp2f(C1[9]); C1[10] = __builtin_amdgcn_exp2f(C1[10]); PIN(C1); SBAR(); \
        N1 = MFMA32(km1, qm, half16<16>(s_)); SBAR(); km1 = lds16(kb + (KO) + 1024); SBAR(); \
        C1[11] = __builtin_amdgcn_exp2f(C1[11]); C1[12] = __builtin_amdgcn_exp2f(C1[12]); C1[13] = __builtin_amdgcn_exp2f(C1[13]); PIN(C1); SBAR(); \
        o = PVMAC(o, v2, oct<0>(C1), idx); SBAR(); v2 = lds16(vb + (VO) + 1408); SBAR(); \
        C1[14] = __builtin_amdgcn_exp2f(C1[14]); C1[15] = __builtin_amdgcn_exp2f(C1[15]); PIN(C1); SBAR(); \
        o = PVMAC(o, v3, oct<8>(C1), idx); SBAR(); v3 = lds16(vb + (VO) + 2112); SBAR(); \
    } while (0)
    for (int j = 0; j < 8; ++j) {
        STEP(A0, A1, B0, B1, 2 * KVPAIR, KVPAIR);
        STEP(B0, B1, A0, A1, 3 * KVPAIR, 2 * KVPAIR);
        if (j <= 5) wait_bar<3>(); else wait_bar<0>();
        if (j <= 4) dma(j + 3);
        const unsigned nx = ((j + 1) & 3) * (unsigned)KVCHUNK;
        kb = kb0 + nx; tb = tb0 + nx;
        STEP(A0, A1, B0, B1, 0, 3 * KVPAIR);
        vb = vb0 + nx;
        STEP(B0, B1, A0, A1, KVPAIR, 0);
    }
#undef STEP
    float l;
    { auto r = __builtin_amdgcn_permlane32_swap(__float_as_uint(o[8]), __float_as_uint(o[8]), false, false); l = __uint_as_float(r[1]); }
    bool ok = l >= 0x1p-40f && l <= 0x1p64f;
#pragma unroll
    for (int r = 0; r < 12; ++r) ok = ok && (__builtin_fabsf(o[r]) < INFINITY || (h == 1 && r > 8));
    if (!__all(ok)) {
        float m = -INFINITY;
        for (int p = 0; p < 32; ++p) {
            const char* pb = kv + (size_t)p * KVPAIR;
            const u32x4 k0 = *(const u32x4*)(pb + lane * 16), k1 = *(const u32x4*)(pb + 1024 + lane * 16); const u32x2 k2 = *(const u32x2*)(pb + 2048 + lane * 8);
            f32x32 s = M2B(k2, qt, (f32x32){});
            const f32x16 s0 = MFMA32(k0, qm, half16<0>(s)), s1 = MFMA32(k1, qm, half16<16>(s));
#pragma unroll
            for (int r = 0; r < 16; ++r) m = __builtin_fmaxf(m, __builtin_fmaxf(s0[r], s1[r]));
        }
        { auto r = __builtin_amdgcn_permlane32_swap(__float_as_uint(m), __float_as_uint(m), false, false); m = __builtin_fmaxf(__uint_as_float(r[0]), __uint_as_float(r[1])); }
        o = (f32x16){};
        for (int p = 0; p < 32; ++p) {
            const char* pb = kv + (size_t)p * KVPAIR;
            const u32x4 k0 = *(const u32x4*)(pb + lane * 16), k1 = *(const u32x4*)(pb + 1024 + lane * 16); const u32x2 k2 = *(const u32x2*)(pb + 2048 + lane * 8);
            const char* pv = pb + 2560 + h * 352 + (t < 22 ? t : 21) * 16;
            f32x32 s = M2B(k2, qt, (f32x32){});
            f32x16 s0 = MFMA32(k0, qm, half16<0>(s)), s1 = MFMA32(k1, qm, half16<16>(s));
#pragma unroll
            for (int r = 0; r < 16; ++r) { s0[r] = __builtin_amdgcn_exp2f(s0[r] - m); s1[r] = __builtin_amdgcn_exp2f(s1[r] - m); }
            o = PVMAC(o, *(const u32x4*)(pv), oct<0>(s0), idx); o = PVMAC(o, *(const u32x4*)(pv + 704), oct<8>(s0), idx);
            o = PVMAC(o, *(const u32x4*)(pv + 1408), oct<0>(s1), idx); o = PVMAC(o, *(const u32x4*)(pv + 2112), oct<8>(s1), idx);
        }
        auto r = __builtin_amdgcn_permlane32_swap(__float_as_uint(o[8]), __float_as_uint(o[8]), false, false); l = __uint_as_float(r[1]);
    }
    const float il = 1.f / l;
    *(u32x4*)(ws + WS_OM + (size_t)(T * 32 + c) * 1024 + lane * 16) = (u32x4){cvtpk(o[0] * il, o[1] * il), cvtpk(o[2] * il, o[3] * il), cvtpk(o[4] * il, o[5] * il), cvtpk(o[6] * il, o[7] * il)};
    if (h == 0) *(u32x2*)(ws + WS_OT + (size_t)(T * 32 + c) * 256 + t * 8) = (u32x2){cvtpk(o[8] * il, o[9] * il), cvtpk(o[10] * il, o[11] * il)};
}

constexpr int PRJ_SLOT = 20 * 1024, PRJ_LDS = 6 * PRJ_SLOT;
template <int MODE>
__global__ __launch_bounds__(256, 1) void k_proj(char* __restrict__ ws, int xin, uint16_t* __restrict__ out) {
    extern __shared__ __attribute__((aligned(16))) char smem[];
    constexpr int NCH = MODE == 0 ? 5 : 12, NS = 4 * NCH, PRE = MODE == 0 ? 5 : 6;
    const int tid = threadIdx.x, lane = tid & 63, w = __builtin_amdgcn_readfirstlane(tid >> 6), t = lane & 15, q = lane >> 4;
    const int xcd = blockIdx.x & 7, id = blockIdx.x >> 3, rb = xcd * 4 + (id >> 3), cb = id & 7;
    const int T = rb * 2 + (w >> 1), tt = 16 * (w & 1) + t;
    const unsigned lds0 = (unsigned)(uintptr_t)smem, voff = lane * 16;
    const char* wsrc = ws + (MODE == 0 ? WS_WO : WS_WD) + (size_t)cb * NS * 5 * 1024 + w * 5 * 1024;
    const char* asrc = ws + (MODE == 0 ? WS_OM + (size_t)T * 32 * 1024 : WS_H + (size_t)T * 96 * 1024);
    const char* tsrc = ws + WS_OT + (size_t)T * 32 * 256;
    const unsigned aoff = (q >> 1) * 1024 + (q & 1) * 512 + tt * 16, toff = q * 512 + tt * 8;
    u32x4 a[NS]; u32x2 ta[8];
    u32x4 r0, r1; u32x2 r2;
    const char* xsrc = ws + WS_X + (size_t)xin * X_BYTES + (size_t)(T * 40 + 5 * cb) * 1024;
    const unsigned xoff2 = (q & 1) * 512 + tt * 16 + (q >> 1) * 8;
    gload16<0>(r0, xsrc, aoff); gload16<2048>(r1, xsrc, aoff); gload8<0>(r2, xsrc + 4096, xoff2);
    auto issue = [&](auto C) { constexpr int ch = C;
        glds<4>(wsrc + ch * PRJ_SLOT, voff, lds0 + (ch % 6) * PRJ_SLOT + w * 5120);
        glds<1>(wsrc + ch * PRJ_SLOT + 4096, voff, lds0 + (ch % 6) * PRJ_SLOT + w * 5120 + 4096);
        if constexpr (MODE == 0 && ch == 4) sfor<0, 8>([&](auto I) { constexpr int i = I; gload8<(i & 1) * 256>(ta[i], tsrc + (i >> 1) * 2048, toff); });
        else sfor<0, 4>([&](auto I) { constexpr int i = I; gload16<(i & 1) * 2048>(a[4 * ch + i], asrc + (4 * ch + (i & 2)) * 2048, aoff); });
    };
    sfor<0, PRE>(issue);
    f32x4 acc[5] = {};
    const unsigned wa = lds0 + voff;
    sfor<0, NCH>([&](auto C) { constexpr int ch = C;
        constexpr int last = MODE == 0 ? 4 : (ch + 4 < 5 ? 5 : (ch + 4 > 11 ? 11 : ch + 4));
        constexpr int N = MODE == 0 ? (ch == 4 ? 0 : 9 * (3 - ch) + 13) : 9 * (last - ch);
        if constexpr (MODE == 0 && ch == 4) {
            asm volatile("s_waitcnt vmcnt(0) lgkmcnt(0)\n\ts_barrier" : "+v"(ta[0]), "+v"(ta[1]), "+v"(ta[2]), "+v"(ta[3]), "+v"(ta[4]), "+v"(ta[5]), "+v"(ta[6]), "+v"(ta[7]) :: "memory");
            sfor<0, 4>([&](auto I) { constexpr int i = I; a[16 + i] = (u32x4){ta[2 * i][0], ta[2 * i][1], ta[2 * i + 1][0], ta[2 * i + 1][1]}; });
        } else
            asm volatile("s_waitcnt vmcnt(%4) lgkmcnt(0)\n\ts_barrier" : "+v"(a[4 * ch]), "+v"(a[4 * ch + 1]), "+v"(a[4 * ch + 2]), "+v"(a[4 * ch + 3]) : "n"(N) : "memory");
        if constexpr (MODE != 0 && ch >= 1 && ch + 5 < NCH) issue(std::integral_constant<int, ch + 5>{});
        sfor<0, 4>([&](auto I) { constexpr int i = I;
            sfor<0, 5>([&](auto Nn) { constexpr int n = Nn;
                acc[n] = MFMA16(lds16(wa + (ch % 6) * PRJ_SLOT + (i * 5 + n) * 1024), a[4 * ch + i], acc[n]);
            });
        });
    });
    asm volatile("s_waitcnt vmcnt(0)" : "+v"(r0), "+v"(r1), "+v"(r2));
    u32x4 y0, y1; u32x2 y2;
#pragma unroll
    for (int d = 0; d < 2; ++d) {
        y0[d]     = cvtpk(bflo(r0[d]) + acc[0][2 * d], bfhi(r0[d]) + acc[0][2 * d + 1]);
        y0[2 + d] = cvtpk(bflo(r0[2 + d]) + acc[1][2 * d], bfhi(r0[2 + d]) + acc[1][2 * d + 1]);
        y1[d]     = cvtpk(bflo(r1[d]) + acc[2][2 * d], bfhi(r1[d]) + acc[2][2 * d + 1]);
        y1[2 + d] = cvtpk(bflo(r1[2 + d]) + acc[3][2 * d], bfhi(r1[2 + d]) + acc[3][2 * d + 1]);
        y2[d]     = cvtpk(bflo(r2[d]) + acc[4][2 * d], bfhi(r2[d]) + acc[4][2 * d + 1]);
    }
    if constexpr (MODE == 2) {
        uint16_t* op = out + (size_t)(32 * T + tt) * DM + 80 * cb;
        const int f0 = 16 * (q >> 1) + 4 * (q & 1);
        *(u32x2*)(op + f0) = (u32x2){y0[0], y0[1]}; *(u32x2*)(op + f0 + 8) = (u32x2){y0[2], y0[3]};
        *(u32x2*)(op + 32 + f0) = (u32x2){y1[0], y1[1]}; *(u32x2*)(op + 32 + f0 + 8) = (u32x2){y1[2], y1[3]};
        *(u32x2*)(op + 64 + 4 * q) = y2;
    } else {
        char* xd = ws + WS_X + (size_t)(xin ^ 1) * X_BYTES + (size_t)(T * 40 + 5 * cb) * 1024;
        *(u32x4*)(xd + aoff) = y0; *(u32x4*)(xd + 2048 + aoff) = y1; *(u32x2*)(xd + 4096 + xoff2) = y2;
        float v[20];
#pragma unroll
        for (int d = 0; d < 4; ++d) { v[2 * d] = bflo(y0[d]); v[2 * d + 1] = bfhi(y0[d]); v[8 + 2 * d] = bflo(y1[d]); v[9 + 2 * d] = bfhi(y1[d]); }
        v[16] = bflo(y2[0]); v[17] = bfhi(y2[0]); v[18] = bflo(y2[1]); v[19] = bfhi(y2[1]);
        float sum = 0.f;
#pragma unroll
        for (int i = 0; i < 20; ++i) sum += v[i];
        sum = swap32_add(swap16_add(sum));
        const float m = sum * (1.f / 80); float m2 = 0.f;
#pragma unroll
        for (int i = 0; i < 20; ++i) m2 += (v[i] - m) * (v[i] - m);
        m2 = swap32_add(swap16_add(m2));
        if (q == 0) *(f32x2*)(ws + WS_ST + (size_t)(xin ^ 1) * ST_BYTES + ((size_t)(32 * T + tt) * 8 + cb) * 8) = (f32x2){sum, m2};
    }
}

constexpr int GU_SLOT = 24 * 1024, GU_LDS = 6 * GU_SLOT + 1024;
__global__ __launch_bounds__(256, 1) void k_gu(char* __restrict__ ws, int xb) {
    extern __shared__ __attribute__((aligned(16))) char smem[];
    const int tid = threadIdx.x, lane = tid & 63, w = __builtin_amdgcn_readfirstlane(tid >> 6), t = lane & 31, h = lane >> 5;
    const int xcd = blockIdx.x & 7, id = blockIdx.x >> 3, rb = (xcd >> 2) * 8 + (id >> 2), cb = (xcd & 3) * 4 + (id & 3);
    const int T = rb * 4 + w;
    const unsigned lds0 = (unsigned)(uintptr_t)smem, voff = lane * 16;
    const char* wsrc = ws + WS_WGU + (size_t)cb * 240 * 1024 + w * 6 * 1024;
    const char* xsrc = ws + WS_X + (size_t)xb * X_BYTES + (size_t)T * 40 * 1024;
    u32x4 a[40], st0, st1;
    const char* ssrc = ws + WS_ST + (size_t)xb * ST_BYTES + (size_t)T * 32 * 64;
    gload16<0>(st0, ssrc, t * 64 + h * 32); gload16<16>(st1, ssrc, t * 64 + h * 32);
    glds<1>(ws + WS_CSG + cb * 768, voff, lds0 + 6 * GU_SLOT);
    auto issue = [&](auto C) { constexpr int ch = C;
        glds<3>(wsrc + ch * GU_SLOT, voff, lds0 + (ch % 6) * GU_SLOT + w * 6144);
        glds<3>(wsrc + ch * GU_SLOT + 3072, voff, lds0 + (ch % 6) * GU_SLOT + w * 6144 + 3072);
        sfor<0, 4>([&](auto I) { constexpr int i = I; gload16<i * 1024>(a[4 * ch + i], xsrc + ch * 4096, voff); });
    };
    sfor<0, 6>(issue);
    f32x16 acc[6] = {};
    const unsigned wa = lds0 + voff;
    sfor<0, 10>([&](auto C) { constexpr int ch = C;
        constexpr int last = ch + 4 < 5 ? 5 : (ch + 4 > 9 ? 9 : ch + 4);
        asm volatile("s_waitcnt vmcnt(%4) lgkmcnt(0)\n\ts_barrier" : "+v"(a[4 * ch]), "+v"(a[4 * ch + 1]), "+v"(a[4 * ch + 2]), "+v"(a[4 * ch + 3]) : "n"(10 * (last - ch)) : "memory");
        if constexpr (ch >= 1 && ch + 5 < 10) issue(std::integral_constant<int, ch + 5>{});
        sfor<0, 4>([&](auto I) { constexpr int i = I;
            sfor<0, 6>([&](auto Nn) { constexpr int n = Nn;
                acc[n] = MFMA32(lds16(wa + (ch % 6) * GU_SLOT + (i * 6 + n) * 1024), a[4 * ch + i], acc[n]);
            });
        });
    });
    asm volatile("" : "+v"(st0), "+v"(st1));
    float mean, rstd; ln_stats(st0, st1, mean, rstd);
    const float r2 = rstd * rstd;
    char* hd = ws + WS_H + (size_t)(T * 96 + cb * 6) * 1024 + lane * 16;
#pragma unroll
    for (int n = 0; n < 6; ++n) {
        float hv[8];
#pragma unroll
        for (int u = 0; u < 2; ++u) {
            const u32x4 cg = lds16(lds0 + 6 * GU_SLOT + n * 128 + u * 64 + h * 16), cu = lds16(lds0 + 6 * GU_SLOT + n * 128 + u * 64 + 32 + h * 16);
#pragma unroll
            for (int i = 0; i < 4; ++i) {
                const float gt = acc[n][8 * u + i] - mean * __uint_as_float(cg[i]), up = acc[n][8 * u + 4 + i] - mean * __uint_as_float(cu[i]);
                hv[4 * u + i] = (gt > 0.f ? gt : 0.f) * up * r2;
            }
        }
        *(u32x4*)(hd + n * 1024) = (u32x4){cvtpk(hv[0], hv[1]), cvtpk(hv[2], hv[3]), cvtpk(hv[4], hv[5]), cvtpk(hv[6], hv[7])};
    }
}

extern "C" void kernel_launch(void* const* d_in, const int* in_sizes, int n_in, void* d_out, int out_size, void* d_ws, size_t ws_size, hipStream_t stream) {
    char* ws = (char*)d_ws;
    (void)hipFuncSetAttribute((const void*)k_qkv, hipFuncAttributeMaxDynamicSharedMemorySize, QKV_LDS);
    (void)hipFuncSetAttribute((const void*)k_attn, hipFuncAttributeMaxDynamicSharedMemorySize, ATT_LDS);
    (void)hipFuncSetAttribute((const void*)k_proj<0>, hipFuncAttributeMaxDynamicSharedMemorySize, PRJ_LDS);
    (void)hipFuncSetAttribute((const void*)k_proj<1>, hipFuncAttributeMaxDynamicSharedMemorySize, PRJ_LDS);
    (void)hipFuncSetAttribute((const void*)k_proj<2>, hipFuncAttributeMaxDynamicSharedMemorySize, PRJ_LDS);
    (void)hipFuncSetAttribute((const void*)k_gu, hipFuncAttributeMaxDynamicSharedMemorySize, GU_LDS);
    k_prep<<<PREP_BLOCKS, 256, 0, stream>>>((const uint16_t*)d_in[0], (const uint16_t*)d_in[1], (const uint16_t*)d_in[2], (const uint16_t*)d_in[4], (const uint16_t*)d_in[5], (const uint16_t*)d_in[6], ws);
    for (int layer = 0; layer < 2; ++layer) {
        k_qkv<<<256, 512, QKV_LDS, stream>>>(ws, 0);
        k_attn<<<256, 512, ATT_LDS, stream>>>(ws);
        k_proj<0><<<256, 256, PRJ_LDS, stream>>>(ws, 0, nullptr);
        k_gu<<<256, 256, GU_LDS, stream>>>(ws, 1);
        if (layer == 0) k_proj<1><<<256, 256, PRJ_LDS, stream>>>(ws, 1, nullptr);
        else k_proj<2><<<256, 256, PRJ_LDS, stream>>>(ws, 1, (uint16_t*)d_out);
    }
}
```

```cpp
#include <hip/hip_runtime.h>
#include <hip/hip_bf16.h>
#include <stdint.h>

typedef __attribute__((ext_vector_type(8))) __bf16 bf16x8;
typedef __attribute__((ext_vector_type(16))) __bf16 bf16x16;
typedef __attribute__((ext_vector_type(4))) short s16x4;
typedef __attribute__((ext_vector_type(2))) float f32x2;
typedef __attribute__((ext_vector_type(4))) float f32x4;
typedef __attribute__((ext_vector_type(8))) float f32x8;
typedef __attribute__((ext_vector_type(16))) float f32x16;
typedef __attribute__((ext_vector_type(32))) float f32x32;
typedef __attribute__((ext_vector_type(2))) unsigned u32x2;
typedef __attribute__((ext_vector_type(4))) unsigned u32x4;
#define LAS __attribute__((address_space(3)))
#define GAS __attribute__((address_space(1)))

constexpr int DM = 640, FF = 1536;
constexpr float LN_EPS = 1e-5f;
constexpr float QSCALE = 0.22360679774997896f * 1.4426950408889634f;

constexpr int QKV_WIN = 3, GU_WIN = 3, WO_WIN = 5, DN_WIN = 5, GU_LD0 = 2;
constexpr int OPEN_Q = 2, OPEN_O = 2, OPEN_G = -1, OPEN_D = 2, KV_SLEEP = 14, RT_SLEEP = 32, POLL_SLEEP = 1;
constexpr int CODE_LEN = 53248;

constexpr unsigned KVTILE = 2688, KVPAIR = 2 * KVTILE, KVCHUNK = 4 * KVPAIR;
constexpr size_t WQ_CS = 80 * 1024, WG_CS = 120 * 1024;
constexpr size_t WQ_BLOB = 85 * 1024, WO_BLOB = 100 * 1024, WG_BLOB = 129 * 1024, WD_BLOB = 240 * 1024;
constexpr size_t WS_FLG  = 0;
constexpr size_t WS_FLK  = 256 * 256, WS_FLW = 2 * 256 * 256;
constexpr size_t WS_WQKV = 3 * 256 * 256;
constexpr size_t WS_WO   = WS_WQKV + 32 * WQ_BLOB;
constexpr size_t WS_WGU  = WS_WO + 8 * WO_BLOB;
constexpr size_t WS_WD   = WS_WGU + 32 * WG_BLOB;
constexpr size_t ST_BYTES = 2048 * 8 * 8, X_BYTES = 64 * 40 * 1024, KV_BYTES = (size_t)32 * 8 * KVCHUNK;
constexpr size_t OM_BYTES = 64 * 32 * 1024, OT_BYTES = 64 * 32 * 256, H_BYTES = 64 * 96 * 1024;
constexpr size_t WS_ST   = WS_WD + 8 * WD_BLOB;
constexpr size_t WS_X    = WS_ST + 4 * ST_BYTES;
constexpr size_t WS_KV   = WS_X + 4 * X_BYTES;
constexpr size_t WS_OM   = WS_KV + 2 * KV_BYTES;
constexpr size_t WS_OT   = WS_OM + 2 * OM_BYTES;
constexpr size_t WS_H    = WS_OT + 2 * OT_BYTES;
static_assert(WS_H + 2 * H_BYTES <= (size_t)256 << 20, "workspace");
constexpr unsigned LDS_BYTES = 163840, L_WQ = KVCHUNK, L_QST = 112640, L_WO = 60416, L_RSC = 139264, L_STAT = 159744, L_CS = L_STAT + 2048, L_CNT = LDS_BYTES - 64;

__device__ __forceinline__ unsigned cvtpk(float lo, float hi) {
    typedef __attribute__((ext_vector_type(2))) __bf16 bf16x2;
    bf16x2 r = {(__bf16)lo, (__bf16)hi};
    return __builtin_bit_cast(unsigned, r);
}
__device__ __forceinline__ float bflo(unsigned u) { return __uint_as_float(u << 16); }
__device__ __forceinline__ float bfhi(unsigned u) { return __uint_as_float(u & 0xffff0000u); }
__device__ __forceinline__ float swap32_add(float v) {
    auto r = __builtin_amdgcn_permlane32_swap(__float_as_uint(v), __float_as_uint(v), false, false);
    return __uint_as_float(r[0]) + __uint_as_float(r[1]);
}
__device__ __forceinline__ float swap16_add(float v) {
    auto r = __builtin_amdgcn_permlane16_swap(__float_as_uint(v), __float_as_uint(v), false, false);
    return __uint_as_float(r[0]) + __uint_as_float(r[1]);
}
__device__ __forceinline__ float relu(float x) { float r; asm("v_max_f32 %0, 0, %1" : "=v"(r) : "v"(x)); return r; }
__device__ __forceinline__ float quad_add(float v) { return swap32_add(swap16_add(v)); }
__device__ __forceinline__ int phi(int s, int h, int j) { return 16 * s + 8 * (j >> 2) + 4 * h + (j & 3); }

#define MFMA32(a, b, c) __builtin_amdgcn_mfma_f32_32x32x16_bf16(__builtin_bit_cast(bf16x8, a), __builtin_bit_cast(bf16x8, b), c, 0, 0, 0)
template <bool FIRST> __device__ __forceinline__ void mfma16(f32x4& acc, const u32x4& a, const u32x4& b) {
    if constexpr (FIRST) asm volatile("v_mfma_f32_16x16x32_bf16 %0, %1, %2, 0" : "=v"(acc) : "v"(a), "v"(b));
    else asm volatile("v_mfma_f32_16x16x32_bf16 %0, %1, %2, %0" : "+v"(acc) : "v"(a), "v"(b));
}
template <int E> __device__ __forceinline__ void fnma2(f32x4& acc, float a, float b, float c, float d) {
    float x = acc[E], y = acc[E + 1];
    asm volatile("v_fma_f32 %0, -%2, %3, %0\n\tv_fma_f32 %1, -%4, %5, %1" : "+v"(x), "+v"(y) : "v"(a), "v"(b), "v"(c), "v"(d));
    acc[E] = x; acc[E + 1] = y;
}
template <int N> __device__ __forceinline__ void mfma_done(f32x4* acc) {
    asm volatile("s_nop 7");
#pragma unroll
    for (int i = 0; i < N; ++i) asm volatile("" : "+v"(acc[i]));
}
#define SBAR() __builtin_amdgcn_sched_barrier(0)
#define PIN(x) asm volatile("" : "+v"(x))
#define FRESH(x) asm volatile("" : "=v"(x))

template <int OFF> __device__ __forceinline__ void gload16(u32x4& r, const char* sbase, unsigned voff) {
    asm volatile("global_load_dwordx4 %0, %1, %2 offset:%3 sc1" : "=v"(r) : "v"(voff), "s"(sbase), "n"(OFF) : "memory");
}
template <int OFF> __device__ __forceinline__ void iload8(u32x2& r, const char* sbase, unsigned voff) {
    asm volatile("global_load_dwordx2 %0, %1, %2 offset:%3" : "=v"(r) : "v"(voff), "s"(sbase), "n"(OFF) : "memory");
}
template <int OFF> __device__ __forceinline__ void gload8(u32x2& r, const char* sbase, unsigned voff) {
    asm volatile("global_load_dwordx2 %0, %1, %2 offset:%3 sc1" : "=v"(r) : "v"(voff), "s"(sbase), "n"(OFF) : "memory");
}
template <int OFF> __device__ __forceinline__ void gstore16(char* sbase, unsigned voff, u32x4 v) {
    asm volatile("global_store_dwordx4 %0, %1, %2 offset:%3 sc1\n\ts_nop 1" :: "v"(voff), "v"(v), "s"(sbase), "n"(OFF) : "memory");
}
template <int OFF> __device__ __forceinline__ void gstore8(char* sbase, unsigned voff, u32x2 v) {
    asm volatile("global_store_dwordx2 %0, %1, %2 offset:%3 sc1" :: "v"(voff), "v"(v), "s"(sbase), "n"(OFF) : "memory");
}
template <int N> __device__ __forceinline__ void glds(const char* sbase, unsigned voff, unsigned lds) {
    if constexpr (N >= 4) {
        asm volatile("s_mov_b32 m0, %2\n\ts_nop 0\n\tglobal_load_lds_dwordx4 %0, %1\n\tglobal_load_lds_dwordx4 %0, %1 offset:1024\n\tglobal_load_lds_dwordx4 %0, %1 offset:2048\n\tglobal_load_lds_dwordx4 %0, %1 offset:3072" :: "v"(voff), "s"(sbase), "s"(lds) : "memory");
        glds<N - 4>(sbase + 4096, voff, lds + 4096);
    }
    if constexpr (N == 1) asm volatile("s_mov_b32 m0, %2\n\ts_nop 0\n\tglobal_load_lds_dwordx4 %0, %1" :: "v"(voff), "s"(sbase), "s"(lds) : "memory");
    if constexpr (N == 2) asm volatile("s_mov_b32 m0, %2\n\ts_nop 0\n\tglobal_load_lds_dwordx4 %0, %1\n\tglobal_load_lds_dwordx4 %0, %1 offset:1024" :: "v"(voff), "s"(sbase), "s"(lds) : "memory");
    if constexpr (N == 3) asm volatile("s_mov_b32 m0, %2\n\ts_nop 0\n\tglobal_load_lds_dwordx4 %0, %1\n\tglobal_load_lds_dwordx4 %0, %1 offset:1024\n\tglobal_load_lds_dwordx4 %0, %1 offset:2048" :: "v"(voff), "s"(sbase), "s"(lds) : "memory");
}
template <int N> __device__ __forceinline__ void wait4(u32x4& a, u32x4& b, u32x4& c, u32x4& d) { asm volatile("s_waitcnt vmcnt(%4)" : "+v"(a), "+v"(b), "+v"(c), "+v"(d) : "n"(N)); }
template <class V> __device__ __forceinline__ void pin2(V& a, V& b) { asm volatile("" : "+v"(a), "+v"(b)); }
template <int N> __device__ __forceinline__ void wait_bar() { asm volatile("s_waitcnt vmcnt(%0) lgkmcnt(0)\n\ts_barrier" :: "n"(N) : "memory"); }
__device__ __forceinline__ void lds_bar() { asm volatile("s_waitcnt lgkmcnt(0)\n\ts_barrier" ::: "memory"); }
template <int I, int N, class F> __device__ __forceinline__ void sfor(F&& f) {
    if constexpr (I < N) { f(std::integral_constant<int, I>{}); sfor<I + 1, N>(f); }
}
__device__ __forceinline__ u32x4 lds16(unsigned a) { return *(const LAS u32x4*)a; }
template <int OFF> __device__ __forceinline__ void ldsr16(u32x4& r, unsigned a) { asm volatile("ds_read_b128 %0, %1 offset:%2" : "=v"(r) : "v"(a), "n"(OFF) : "memory"); }
template <int OFF> __device__ __forceinline__ void ldsrf(f32x4& r, unsigned a) { asm volatile("ds_read_b128 %0, %1 offset:%2" : "=v"(r) : "v"(a), "n"(OFF) : "memory"); }
template <int OFF> __device__ __forceinline__ void ldsrf(float& r, unsigned a) { asm volatile("ds_read_b32 %0, %1 offset:%2" : "=v"(r) : "v"(a), "n"(OFF) : "memory"); }
template <int OFF> __device__ __forceinline__ void ldsw16(unsigned a, const f32x4& v) { asm volatile("ds_write_b128 %0, %1 offset:%2" :: "v"(a), "v"(v), "n"(OFF) : "memory"); }
__device__ __forceinline__ u32x2 lds8(unsigned a) { return *(const LAS u32x2*)a; }

__device__ __forceinline__ void transpose8x4(const u32x2 (&r)[8], u32x4 (&o)[4]) {
#pragma unroll
    for (int d = 0; d < 4; ++d) {
        o[0][d] = __builtin_amdgcn_perm(r[2 * d + 1][0], r[2 * d][0], 0x05040100u);
        o[1][d] = __builtin_amdgcn_perm(r[2 * d + 1][0], r[2 * d][0], 0x07060302u);
        o[2][d] = __builtin_amdgcn_perm(r[2 * d + 1][1], r[2 * d][1], 0x05040100u);
        o[3][d] = __builtin_amdgcn_perm(r[2 * d + 1][1], r[2 * d][1], 0x07060302u);
    }
}
__device__ __forceinline__ void qkv_slot(int which, int dg, int& n, int& qq) {
    if (dg == 4) { n = 2; qq = which; }
    else if (which == 2) { n = 3; qq = dg; }
    else { n = dg >> 1; qq = 2 * which + (dg & 1); }
}
__device__ __forceinline__ void proj_slot(int rr, int& n, int& qq) {
    const int sl = rr >> 2, gi = rr & 3;
    n = sl < 4 ? 2 * (sl >> 1) + (gi >> 1) : 4; qq = sl < 4 ? 2 * (sl & 1) + (gi & 1) : gi;
}
__device__ __forceinline__ f32x4 colsum8(const u32x4 (&o)[4]) {
    typedef __attribute__((ext_vector_type(2))) __bf16 bf16x2;
    const bf16x2 one = __builtin_bit_cast(bf16x2, 0x3f803f80u);
    f32x4 cs = {};
#pragma unroll
    for (int i = 0; i < 4; ++i)
#pragma unroll
        for (int d = 0; d < 4; ++d) { const unsigned x = o[i][d]; cs[i] = __builtin_amdgcn_fdot2_f32_bf16(__builtin_bit_cast(bf16x2, x), one, cs[i], false); }
    return cs;
}
__device__ __forceinline__ void pstore16(char* p, u32x4 v) {
    asm volatile("global_store_dwordx4 %0, %1, off sc1\n\ts_nop 1" :: "v"(p), "v"(v) : "memory");
}
constexpr int PB_X = 128, PB_QKV = 160, PB_O = 80, PB_Z = 48 + 96;
constexpr int PREP_BLOCKS = PB_X + PB_QKV + PB_O + PB_Z;

__global__ __launch_bounds__(256) void k_prep(const uint16_t* __restrict__ x, const uint16_t* __restrict__ wqkv, const uint16_t* __restrict__ wo, char* __restrict__ ws) {
    __shared__ __attribute__((aligned(16))) char img[16 * 1024 + 4 * 1024];
    u32x4 pf; unsigned pfo;
    asm volatile("s_getpc_b64 s[20:21]\n\tv_lshlrev_b32 %1, 4, %2\n\ts_and_b32 s20, s20, 0xffffff00\n\tglobal_load_dwordx4 %0, %1, s[20:21]" : "=&v"(pf), "=&v"(pfo) : "v"(threadIdx.x) : "s20", "s21", "scc", "memory");
#define PF_DONE() asm volatile("s_waitcnt vmcnt(0)" : "+v"(pf))
    int b = blockIdx.x; const int tid = threadIdx.x, lane = tid & 63, q = tid >> 6;
    if (b < PB_X) {
        const int wv = b * 4 + q, T = wv >> 3, cb = wv & 7, t = lane & 31, h = lane >> 5;
        const uint16_t* src = x + (size_t)(32 * T + t) * DM + 80 * cb + 8 * h;
        u32x4 v[5];
#pragma unroll
        for (int i = 0; i < 5; ++i) v[i] = *(const u32x4*)(src + 16 * i);
        asm volatile("" : "+v"(v[0]), "+v"(v[1]), "+v"(v[2]), "+v"(v[3]), "+v"(v[4]));
#pragma unroll
        for (int i = 0; i < 5; ++i) {
            auto s0 = __builtin_amdgcn_permlane32_swap(v[i][0], v[i][2], false, false);
            auto s1 = __builtin_amdgcn_permlane32_swap(v[i][1], v[i][3], false, false);
            pstore16(ws + WS_X + ((size_t)(T * 40 + 5 * cb + i) * 64 + lane) * 16, (u32x4){s0[0], s1[0], s0[1], s1[1]});
        }
        float sum = 0.f;
#pragma unroll
        for (int i = 0; i < 5; ++i)
#pragma unroll
            for (int d = 0; d < 4; ++d) sum += bflo(v[i][d]) + bfhi(v[i][d]);
        sum = swap32_add(sum);
        const float m = sum * (1.f / 80); float m2 = 0.f;
#pragma unroll
        for (int i = 0; i < 5; ++i)
#pragma unroll
            for (int d = 0; d < 4; ++d) { const float a = bflo(v[i][d]) - m, c = bfhi(v[i][d]) - m; m2 += a * a + c * c; }
        m2 = swap32_add(m2);
        if (h == 0) *(f32x2*)(ws + WS_ST + ((size_t)(32 * T + t) * 8 + cb) * 8) = (f32x2){sum, m2};
 PF_DONE(); return;
    }
    b -= PB_X;
    if (b >= PB_QKV + PB_O) {
        const int z = b - (PB_QKV + PB_O);
        if (z < 48) pstore16(ws + WS_FLG + (size_t)(z * 256 + tid) * 16, (u32x4){0, 0, 0, 0});
        else pstore16(ws + WS_ST + ST_BYTES + (size_t)((z - 48) * 256 + tid) * 16, (u32x4){~0u, ~0u, ~0u, ~0u});
 PF_DONE(); return;
    }
    const bool isq = b < PB_QKV;
    int S, cg, blk, n, qq; size_t dst;
    if (isq) {
        S = b >> 3; const int un = b & 7, which = lane / 20, r = lane % 20; blk = r / 5;
        cg = which * 160 + un * 20 + r; qkv_slot(which, r % 5, n, qq); dst = WS_WQKV + (size_t)un * 4 * WQ_BLOB;
    } else {
        b -= PB_QKV; S = b >> 2; const int un = b & 3; blk = lane / 20;
        cg = un * 40 + lane; proj_slot(lane % 20, n, qq); dst = WS_WO + (size_t)un * 2 * WO_BLOB;
    }
    const int nt = isq ? 4 : 5; const size_t blob = isq ? WQ_BLOB : WO_BLOB;
    const unsigned l0 = (unsigned)(uintptr_t)img;
    if (lane < (isq ? 60 : 40)) {
        u32x2 r[8]; u32x4 o[4];
        const bool tail = !isq && S >= 16;
        const int k0 = isq ? phi(2 * S + (q >> 1), q & 1, 0) : (tail ? 20 * (8 * (S - 16) + 2 * q) + 16 : 20 * (2 * S + (q >> 1)) + 4 * (q & 1)), kh = tail ? 20 : 8;
        const unsigned ld = isq ? 6 * DM : 2 * DM;
        const char* src = (const char*)wqkv + (isq ? 0 : (const char*)wo - (const char*)wqkv) + (size_t)k0 * ld + 8 * cg;
#pragma unroll
        for (int j = 0; j < 8; ++j) r[j] = *(const u32x2*)(src + ((j >> 2) * kh + (j & 3)) * ld);
        transpose8x4(r, o);
#pragma unroll
        for (int i = 0; i < 4; ++i) *(LAS u32x4*)(l0 + (blk * nt + n) * 1024 + (q * 16 + 4 * qq + i) * 16) = o[i];
        if (isq) *(LAS f32x4*)(l0 + 16384 + q * 1024 + ((blk * 4 + n) * 16 + 4 * qq) * 4) = colsum8(o);
    } else if (isq) {
#pragma unroll
        for (int i = 0; i < 4; ++i) *(LAS u32x4*)(l0 + ((lane - 60) * 4 + 2) * 1024 + (q * 16 + 12 + i) * 16) = (u32x4){0, 0, 0, 0};
        *(LAS f32x4*)(l0 + 16384 + q * 1024 + (((lane - 60) * 4 + 2) * 16 + 12) * 4) = (f32x4){};
    }
    __syncthreads();
#pragma unroll
    for (int i = 0; i < 4; ++i) {
        const int f = q + 4 * i, fb = f / nt;
        if (isq || f < 10) pstore16(ws + dst + fb * blob + (size_t)(S * nt + f - fb * nt) * 1024 + lane * 16, lds16(l0 + f * 1024 + lane * 16));
    }
    if (isq) {
        const LAS float* cp = (const LAS float*)(l0 + 16384) + tid;
        *(float*)(ws + dst + q * WQ_BLOB + WQ_CS + S * 256 + lane * 4) = (cp[0] + cp[256]) + (cp[512] + cp[768]);
    }
 PF_DONE();
}

#define M2B(kt, qt, c) __builtin_amdgcn_mfma_f32_32x32x4bf16_1k(__builtin_bit_cast(s16x4, kt), __builtin_bit_cast(s16x4, qt), c, 0, 0, 0)
template <int O> __device__ __forceinline__ f32x16 half16(const f32x32& s) {
    return __builtin_shufflevector(s, s, O, O + 1, O + 2, O + 3, O + 4, O + 5, O + 6, O + 7, O + 8, O + 9, O + 10, O + 11, O + 12, O + 13, O + 14, O + 15);
}
template <int O> __device__ __forceinline__ f32x8 oct(const f32x16& s) { return __builtin_shufflevector(s, s, O, O + 1, O + 2, O + 3, O + 4, O + 5, O + 6, O + 7); }
#define PVMAC(o, v, p8, idx) __builtin_amdgcn_smfmac_f32_32x32x32_bf16(__builtin_bit_cast(bf16x8, v), __builtin_bit_cast(bf16x16, p8), o, idx, 0, 0)

typedef __attribute__((ext_vector_type(32))) unsigned u32x32;
struct Park { u32x32 a, b, c; };
__device__ __forceinline__ void unpark(const u32x32& p, unsigned d) {
    sfor<0, 8>([&](auto I_) { constexpr int i = I_; *(LAS u32x4*)(d + i * 1024) = __builtin_shufflevector(p, p, 4 * i, 4 * i + 1, 4 * i + 2, 4 * i + 3); });
}
struct Res { u32x4 r0, r1; u32x2 r2, st; };
struct Ctx { char* ws; int lane, w, g, m; unsigned lds0, voff; };
__device__ __forceinline__ Ctx fresh(const Ctx& c) {
    Ctx r = c; asm volatile("" : "+s"(r.ws), "+s"(r.w), "+s"(r.g), "+s"(r.m), "+s"(r.lds0));
    asm volatile("v_mbcnt_lo_u32_b32 %0, -1, 0\n\tv_mbcnt_hi_u32_b32 %0, -1, %0" : "=v"(r.lane)); r.voff = r.lane * 16; return r;
}
__device__ __forceinline__ void bar() { asm volatile("s_barrier" ::: "memory"); }
__device__ __forceinline__ bool drain_last(const Ctx& c) {
    asm volatile("s_waitcnt vmcnt(0)" ::: "memory");
    unsigned old = 0;
    if (c.lane == 0) old = __hip_atomic_fetch_add((LAS unsigned*)(c.lds0 + L_CNT), 1u, __ATOMIC_RELAXED, __HIP_MEMORY_SCOPE_WORKGROUP);
    return (__builtin_amdgcn_readfirstlane(old) & 3) == 3;
}
__device__ __forceinline__ void poll(GAS unsigned* p, unsigned epoch) {
    for (unsigned spin = 0; spin < (1u << 20); ++spin) {
        if (__all(__hip_atomic_load(p, __ATOMIC_RELAXED, __HIP_MEMORY_SCOPE_AGENT) >= epoch)) break;
        __builtin_amdgcn_s_sleep(POLL_SLEEP);
    }
}
template <int SLEEP> __device__ __forceinline__ void open_phase() {
    if (SLEEP >= 0) asm volatile("s_waitcnt vmcnt(0)" ::: "memory");
    if (SLEEP > 0) __builtin_amdgcn_s_sleep(SLEEP);
}
__device__ __forceinline__ unsigned max3u(unsigned a, unsigned b, unsigned c) { return max(max(a, b), c); }
__device__ __forceinline__ void chk_first(unsigned m, unsigned a, unsigned b, unsigned c) { asm volatile("v_max3_u32 %0, %1, %2, %3" :: "v"(m), "v"(a), "v"(b), "v"(c)); }
__device__ __forceinline__ void chk_more(unsigned m, unsigned a, unsigned b) { asm volatile("v_max3_u32 %0, %0, %1, %2" :: "v"(m), "v"(a), "v"(b)); }
__device__ __forceinline__ void chk_last(unsigned long long& k, unsigned m, unsigned a) { asm volatile("v_max_u32 %1, %1, %2\n\tv_cmp_eq_u32_e64 %0, -1, %1" : "=s"(k) : "v"(m), "v"(a)); }
template <int N, int L> __device__ __forceinline__ void waitc() { asm volatile("s_waitcnt vmcnt(%0) lgkmcnt(%1)" :: "n"(N), "n"(L)); }
__device__ __forceinline__ bool stale(unsigned m) { return __builtin_amdgcn_ballot_w64(m == ~0u) != 0; }
#define RARE(x) __builtin_expect(x, 0)
__device__ __forceinline__ void put_stats(const Ctx& c, const Res& res, int xbuf) {
    const int T = c.g * 8 + (c.m >> 3) * 2 + (c.w & 1);
    if (c.lane < 16) gstore8<0>(c.ws + WS_ST + (size_t)xbuf * ST_BYTES + (size_t)T * 2048 + (c.m & 7) * 8, (16 * (c.w >> 1) + c.lane) * 64, res.st);
}
template <int NT> struct Stats { u32x4 p[4], v[5]; const char* src; };
template <int NT> __device__ __forceinline__ void stats_load(const Ctx& c_, Stats<NT>& st, int xbuf) {
    const Ctx c = fresh(c_);
    st.src = c.ws + WS_ST + (size_t)xbuf * ST_BYTES + (size_t)(c.g * 256 + (c.w - 4) * 64) * 64;
    const unsigned off = (c.lane & 15) * 64 + (c.lane >> 4) * 16;
    sfor<0, 4>([&](auto U_) { constexpr int u = U_; gload16<u * 1024>(st.p[u], st.src, off); });
    if constexpr (NT > 0) {
        const char* cs = c.ws + WS_WQKV + WQ_CS + c.m * WQ_BLOB;
        const unsigned co = (c.lane >> 4) * 1280 + (c.lane & 15) * 16;
#pragma unroll
        for (int i = 0; i < 5; ++i) FRESH(st.v[i]);
        if (c.w == 7) sfor<0, 5>([&](auto I_) { constexpr int i = I_; gload16<i * 256>(st.v[i], cs, co); });
    }
}
template <int NT> __device__ __forceinline__ void stats_finish(const Ctx& c_, Stats<NT>& st) {
    const Ctx c = fresh(c_);
#pragma unroll
    for (int u = 0; u < 4; ++u) PIN(st.p[u]);
    for (unsigned spin = 0; spin < (1u << 20); ++spin) {
        unsigned mx = 0;
#pragma unroll
        for (int u = 0; u < 4; ++u) mx = max(max(mx, max(st.p[u][0], st.p[u][1])), max(st.p[u][2], st.p[u][3]));
        if (!__any(mx == ~0u)) break;
        const unsigned off = (c.lane & 15) * 64 + (c.lane >> 4) * 16;
        gload16<0>(st.p[0], st.src, off); gload16<1024>(st.p[1], st.src, off); gload16<2048>(st.p[2], st.src, off); gload16<3072>(st.p[3], st.src, off);
        asm volatile("s_waitcnt vmcnt(0)" : "+v"(st.p[0]), "+v"(st.p[1]), "+v"(st.p[2]), "+v"(st.p[3]));
    }
#pragma unroll
    for (int u = 0; u < 4; ++u) {
        const float s0 = __uint_as_float(st.p[u][0]), s1 = __uint_as_float(st.p[u][2]), mean = quad_add(s0 + s1) * (1.f / DM);
        const float d0 = s0 * (1.f / 80) - mean, d1 = s1 * (1.f / 80) - mean;
        const float m2 = quad_add((__uint_as_float(st.p[u][1]) + __uint_as_float(st.p[u][3])) + 80.f * (d0 * d0 + d1 * d1));
        if (c.lane < 32) *(LAS float*)(c.lds0 + L_STAT + (c.lane >> 4) * 1024 + ((c.w - 4) * 64 + 16 * u + (c.lane & 15)) * 4) = c.lane < 16 ? mean : __builtin_amdgcn_rsqf(m2 * (1.f / DM) + LN_EPS);
    }
    if constexpr (NT > 0) {
        f32x4 sum = {};
        if (c.w == 7) {
#pragma unroll
            for (int i = 0; i < 5; ++i) { PIN(st.v[i]); sum += __builtin_bit_cast(f32x4, st.v[i]); }
#pragma unroll
            for (int i = 0; i < 4; ++i) sum[i] = quad_add(sum[i]);
            if (c.lane < 4 * NT) *(LAS f32x4*)(c.lds0 + L_CS + c.voff) = sum;
        }
    }
}

template <int NT, int WIN, int TR, int LD0, class P, class H>
__device__ __forceinline__ void gemm_tok64(const Ctx& c, const char* xsrc, unsigned wl, f32x4 (&acc)[4][NT], P&& pre, H&& hook) {
    const int t = c.lane & 15, q = c.lane >> 4;
    unsigned aoff = (q >> 1) * 1024 + (q & 1) * 512 + t * 16, aoff2 = aoff + 40960;
    const int rot = NT == 4 ? (c.m & 1) * 10 : 0;
    unsigned wa0 = wl + c.voff + rot * NT * 1024, wa1 = wl + c.voff + (10 - rot) * NT * 1024;
    const char* xs0 = xsrc + rot * 2048; const char* xs1 = xsrc + (10 - rot) * 2048;
    PIN(aoff); PIN(aoff2); PIN(wa0); PIN(wa1); asm volatile("" : "+s"(xs0), "+s"(xs1));
    pre(0);
    open_phase<(NT == 4 ? OPEN_Q : OPEN_G)>();
    u32x4 a[20][4];
    auto issue = [&](auto S_, auto U_) { constexpr int S = S_, u = U_;
        gload16<(S & 1) * 2048 + (u & 1) * 256>(a[S][u], (S < 10 ? xs0 : xs1) + ((S % 10) >> 1) * 4096, u < 2 ? aoff : aoff2); };
    sfor<0, WIN>([&](auto S_) { sfor<0, 4>([&](auto U_) { issue(S_, U_); }); });
    pre(1);
    lds_bar();
    u32x4 w[NT];
    auto wread = [&](auto S_, auto N_) { constexpr int S = S_, n = N_; ldsr16<((S % 10) * NT + n) * 1024>(w[n], S < 10 ? wa0 : wa1); };
    sfor<0, NT - 1>([&](auto N_) { wread(std::integral_constant<int, 0>{}, N_); });
    unsigned mx; FRESH(mx); unsigned long long bad;
    auto again = [&](auto S_) { constexpr int S = S_;
        for (unsigned spin = 0; spin < (1u << 18); ++spin) {
            constexpr int E = S + WIN < 20 ? S + WIN : 20;
            waitc<0, 15>();
            sfor<S, E>([&](auto R_) { sfor<0, 4>([&](auto U_) { issue(R_, U_); }); });
            wait4<4 * (E - S - 1)>(a[S][0], a[S][1], a[S][2], a[S][3]);
            if (!stale(max3u(max3u(max3u(a[S][0][0], a[S][0][2], a[S][1][0]), a[S][1][2], a[S][2][0]), a[S][2][2], max(a[S][3][0], a[S][3][2])))) break;
        } };
    wait4<4 * (WIN - 1)>(a[0][0], a[0][1], a[0][2], a[0][3]);
    if (RARE(stale(max3u(max3u(max3u(a[0][0][0], a[0][0][2], a[0][1][0]), a[0][1][2], a[0][2][0]), a[0][2][2], max(a[0][3][0], a[0][3][2]))))) again(std::integral_constant<int, 0>{});
    sfor<0, 20>([&](auto S_) { constexpr int S = S_, S1 = S < 19 ? S + 1 : 19, GW = 4 * (NT - 2) - 1;
        waitc<63, 1>();
        SBAR();
        sfor<0, 4 * NT>([&](auto G_) { constexpr int g = G_, n = g >> 2, u = g & 3;
            if constexpr (n == TR) mfma16<S == 0>(acc[u][n], a[S][u], w[n]); else mfma16<S == 0>(acc[u][n], w[n], a[S][u]);
            SBAR();
            if constexpr (g == 0) wread(S_, std::integral_constant<int, NT - 1>{});
            else if constexpr (u == 0) { if constexpr (S < 19) wread(std::integral_constant<int, S + 1>{}, std::integral_constant<int, (n + NT - 1) % NT>{}); }
            else if constexpr (u == 2) { if constexpr (n >= LD0 && n < LD0 + 4 && S + WIN < 20) issue(std::integral_constant<int, (S + WIN < 20 ? S + WIN : 0)>{}, std::integral_constant<int, (n - LD0) & 3>{}); }
            else hook(S_, G_);
            if constexpr (g == GW) {
                constexpr int full = S + WIN - 1 < 19 ? S + WIN - 1 : 19, nb = NT - 2 - LD0 < 4 ? NT - 2 - LD0 : 4;
                if constexpr (S < 19) waitc<4 * (full - S1) + (S + WIN < 20 ? nb : 0), NT - 3>(); else waitc<63, 0>();
            }
            if constexpr (S < 19) {
                if constexpr (g == GW + 2) chk_first(mx, a[S1][0][0], a[S1][0][2], a[S1][1][0]);
                if constexpr (g == GW + 3) chk_more(mx, a[S1][1][2], a[S1][2][0]);
                if constexpr (g == GW + 4) chk_more(mx, a[S1][2][2], a[S1][3][0]);
                if constexpr (g == GW + 5) chk_last(bad, mx, a[S1][3][2]);
                if constexpr (g == GW + 8) { if (RARE(bad != 0)) again(std::integral_constant<int, S1>{}); }
            }
            SBAR();
        });
    });
    mfma_done<4 * NT>(&acc[0][0]);
}

__device__ __forceinline__ void fetch_res(Res& r, int layer, const char* xs, unsigned o, unsigned o2) {
    asm volatile("s_cmp_lg_u32 %3, 0\n\ts_cbranch_scc1 1f\n\tglobal_load_dwordx4 %0, %4, %6\n\tglobal_load_dwordx4 %1, %4, %6 offset:2048\n\tglobal_load_dwordx2 %2, %5, %6\n1:"
                 : "+v"(r.r0), "+v"(r.r1), "+v"(r.r2) : "s"(layer), "v"(o), "v"(o2), "s"(xs) : "scc", "memory");
}
__device__ __forceinline__ void phase_qkv_attn(const Ctx& c_, int layer, Res& res, Park& pk) {
    const Ctx c = fresh(c_);
    char* ws = c.ws; const int lane = c.lane, w = c.w, T = c.g * 8 + 2 * w;
    const unsigned lds0 = c.lds0, qb = lds0 + L_QST + w * 2560;
    {
        const int t = lane & 15, q = lane >> 4;
        f32x4 acc[4][4], cs[3], mv[4], rv[4]; float cs3, mean[4], r01[4], r2[4];
        const unsigned pb = lds0 + w * KVPAIR;
        unsigned a1 = (q < 2 ? qb : pb) + (q & 1) * 512 + t * 16, a1b = a1 + (q < 2 ? 1280 : KVTILE);
        unsigned a2 = (q == 0 ? qb + 1024 : pb + 2432) + t * 8, a2b = a2 + (q == 0 ? 1280 : KVTILE);
        unsigned a3 = pb + 1024 + (q >> 1) * 352 + t * 16 + (q & 1) * 8, a4 = pb + 1024 + 256 + (t >> 3) * 352 + (t & 7) * 2;
        unsigned sa = lds0 + L_STAT + w * 256 + t * 4, sv = lds0 + L_STAT + w * 256 + q * 16, ca = lds0 + L_CS + q * 16, c3 = lds0 + L_CS + 192 + t * 4;
        const char* xs = ws + WS_X + (size_t)((c.g * 8 + (c.m >> 3) * 2 + (w & 1)) * 40 + 5 * (c.m & 7)) * 1024;
        unsigned ro = (q >> 1) * 1024 + (q & 1) * 512 + (16 * (w >> 1) + t) * 16, ro2 = 4096 + (q & 1) * 512 + (16 * (w >> 1) + t) * 16 + (q >> 1) * 8; PIN(ro); PIN(ro2);
        float s01 = q < 2 ? QSCALE : 1.f, s2 = q == 0 ? QSCALE : 1.f; PIN(s01); PIN(s2); PIN(c3);
        PIN(a1); PIN(a1b); PIN(a2); PIN(a2b); PIN(a3); PIN(a4); PIN(sa); PIN(sv); PIN(ca);
        gemm_tok64<4, QKV_WIN, 3, 0>(c, ws + WS_X + (size_t)(2 * layer) * X_BYTES + (size_t)T * 40 * 1024, lds0 + L_WQ, acc, [&](int k) { if (k == 0 && layer) put_stats(c, res, 2); }, [&](auto S_, auto G_) { constexpr int S = S_, g = G_, j = g >> 1;
            if constexpr (S == 1 && j == 0) {
                if (lane < 16) { const unsigned one = (lane & 1) ? 0u : 0x3f803f80u; *(LAS u32x4*)(pb + (lane >> 3) * KVTILE + 1024 + ((lane >> 1) & 3) * 352 + (20 + (lane & 1)) * 16) = (u32x4){one, one, one, one}; }
            }
            if constexpr (S == 12 && j == 0) fetch_res(res, layer, xs, ro, ro2);
            if constexpr (S == 5 && j == 0) bar();
            if constexpr (S == 9 && j == 0) bar();
            if constexpr (S == 10 && j >= 1 && j < 4) ldsrf<((j - 1) & 3) * 64>(cs[(j - 1) % 3], ca);
            if constexpr (S == 10 && j == 4) ldsrf<0>(cs3, c3);
            if constexpr (S == 11 && j < 4) ldsrf<j * 64>(mean[j & 3], sa);
            if constexpr (S == 11 && j >= 4) ldsrf<(j & 3) * 64>(mv[j & 3], sv);
            if constexpr (S >= 13 && S < 17) { constexpr int u = (S - 13) & 3, k = ((g + 7) & 15) >> 2, e = g & 2;
                if constexpr (k == 3) fnma2<e>(acc[u][3], mv[u][e], cs3, mv[u][e + 1], cs3); else fnma2<e>(acc[u][k], mean[u], cs[k % 3][e], mean[u], cs[k % 3][e + 1]);
            }
            if constexpr (S == 17 && j < 4) r01[j & 3] = *(const LAS float*)(sa + 1024 + j * 64);
            if constexpr (S == 17 && j >= 4) rv[j & 3] = *(const LAS f32x4*)(sv + 1024 + (j & 3) * 64);
            if constexpr (S == 19 && j < 4) { r2[j & 3] = r01[j & 3] * s2; PIN(r2[j & 3]); }
            if constexpr (S == 19 && j >= 4) { r01[j & 3] *= s01; PIN(r01[j & 3]); }
        });
#pragma unroll
        for (int b = 0; b < 2; ++b) {
            u32x2 p2[2];
#pragma unroll
            for (int e = 0; e < 2; ++e) {
                const int u = 2 * b + e;
                const f32x4 y0 = acc[u][0] * r01[u], y1 = acc[u][1] * r01[u], y2 = acc[u][2] * r2[u], y3 = acc[u][3] * rv[u];
                *(LAS u32x4*)((b ? a1b : a1) + e * 256) = (u32x4){cvtpk(y0[0], y0[1]), cvtpk(y0[2], y0[3]), cvtpk(y1[0], y1[1]), cvtpk(y1[2], y1[3])};
                *(LAS u32x2*)(a3 + b * KVTILE + e * 704) = (u32x2){cvtpk(y3[0], y3[1]), cvtpk(y3[2], y3[3])};
                p2[e] = (u32x2){cvtpk(y2[0], y2[1]), cvtpk(y2[2], y2[3])};
            }
            if (q < 2) { *(LAS u32x2*)(b ? a2b : a2) = p2[0]; *(LAS u32x2*)((b ? a2b : a2) + 128) = p2[1]; }
            else if (q == 2) {
#pragma unroll
                for (int e = 0; e < 2; ++e) {
                    const unsigned a = a4 + b * KVTILE + e * 704;
                    *(LAS uint16_t*)(a) = (uint16_t)p2[e][0]; *(LAS uint16_t*)(a + 16) = (uint16_t)(p2[e][0] >> 16);
                    *(LAS uint16_t*)(a + 32) = (uint16_t)p2[e][1]; *(LAS uint16_t*)(a + 48) = (uint16_t)(p2[e][1] >> 16);
                }
            }
            lds_bar();
        }
    }
    const int t = lane & 31, h = lane >> 5;
    u32x4 qm0 = lds16(qb + c.voff), qm1 = lds16(qb + 1280 + c.voff);
    u32x2 qt0 = lds8(qb + 1024 + t * 8), qt1 = lds8(qb + 1280 + 1024 + t * 8);
    f32x16 o0, o1;
    char* om = ws + WS_OM + layer * OM_BYTES + (size_t)(T * 32 + c.m) * 1024; char* ot = ws + WS_OT + layer * OT_BYTES + (size_t)(T * 32 + c.m) * 256;
    asm volatile("" : "+s"(om), "+s"(ot));
    {
        const char* wpk = ws + WS_WGU + c.m * WG_BLOB + w * 8192;
        const unsigned kb = lds0 + lane * 16, tb = lds0 + h * KVTILE + 2432 + t * 8, vb = lds0 + h * 352 + (t < 21 ? t : 21) * 16;
        asm volatile(
            "ds_read_b64 v[116:117], v139 offset:0\n"
            "ds_read_b128 v[108:111], v138 offset:0\n"
            "ds_read_b128 v[112:115], v138 offset:2688\n"
            "ds_read_b128 v[118:121], v140 offset:1024\n"
            "ds_read_b128 v[122:125], v140 offset:1728\n"
            "ds_read_b128 v[126:129], v140 offset:3712\n"
            "ds_read_b128 v[130:133], v140 offset:4416\n"
            "v_mov_b32 v134, 0xdddddddd\n"
            "s_mov_b32 s62, 0\n"
            "s_mov_b32 s63, 0\n"
            "v_mov_b32 v56, 0\n"
            "v_mov_b32 v57, 0\n"
            "v_mov_b32 v58, 0\n"
            "v_mov_b32 v59, 0\n"
            "v_mov_b32 v60, 0\n"
            "v_mov_b32 v61, 0\n"
            "v_mov_b32 v62, 0\n"
            "v_mov_b32 v63, 0\n"
            "s_waitcnt lgkmcnt(6)\n"
            "v_mfma_f32_32x32x4_2b_bf16 v[0:31], v[116:117], v[100:101], 0\n"
            "v_mov_b32 v64, 0\n"
            "v_mov_b32 v65, 0\n"
            "v_mov_b32 v66, 0\n"
            "v_mov_b32 v67, 0\n"
            "v_mov_b32 v68, 0\n"
            "v_mov_b32 v69, 0\n"
            "v_mov_b32 v70, 0\n"
            "v_mov_b32 v71, 0\n"
            "v_mov_b32 v72, 0\n"
            "v_mov_b32 v73, 0\n"
            "v_mov_b32 v74, 0\n"
            "v_mov_b32 v75, 0\n"
            "v_mov_b32 v76, 0\n"
            "v_mov_b32 v77, 0\n"
            "v_mov_b32 v78, 0\n"
            "v_mov_b32 v79, 0\n"
            "v_mov_b32 v80, 0\n"
            "v_mov_b32 v81, 0\n"
            "v_mov_b32 v82, 0\n"
            "v_mov_b32 v83, 0\n"
            "s_waitcnt lgkmcnt(4)\n"
            "v_mfma_f32_32x32x16_bf16 v[0:15], v[108:111], v[96:99], v[0:15]\n"
            "v_mfma_f32_32x32x16_bf16 v[16:31], v[112:115], v[96:99], v[16:31]\n"
            "v_mov_b32 v84, 0\n"
            "v_mov_b32 v85, 0\n"
            "v_mov_b32 v86, 0\n"
            "v_mov_b32 v87, 0\n"
            "v_mov_b32 v88, 0\n"
            "v_mov_b32 v89, 0\n"
            "v_mov_b32 v90, 0\n"
            "v_mov_b32 v91, 0\n"
            "v_mov_b32 v92, 0\n"
            "v_mov_b32 v93, 0\n"
            "v_mov_b32 v94, 0\n"
            "v_mov_b32 v95, 0\n"
            "s_waitcnt lgkmcnt(0)\n"
            ".p2align 3\n"
            "2:\n"
            "v_exp_f32 v0, v0\n"
            "v_exp_f32 v1, v1\n"
            "v_exp_f32 v2, v2\n"
            "v_smfmac_f32_32x32x32_bf16 v[80:95], v[130:133], v[56:63], v134\n"
            "ds_read_b128 v[130:133], v140 offset:4416\n"
            "v_exp_f32 v3, v3\n"
            "v_exp_f32 v4, v4\n"
            "v_exp_f32 v5, v5\n"
            "v_mfma_f32_32x32x4_2b_bf16 v[32:63], v[116:117], v[106:107], 0\n"
            "ds_read_b64 v[116:117], v139 offset:5376\n"
            "v_exp_f32 v6, v6\n"
            "v_exp_f32 v7, v7\n"
            "v_exp_f32 v8, v8\n"
            "v_exp_f32 v9, v9\n"
            "v_exp_f32 v10, v10\n"
            "v_exp_f32 v11, v11\n"
            "v_exp_f32 v12, v12\n"
            "v_exp_f32 v13, v13\n"
            "s_waitcnt lgkmcnt(1)\n"
            "v_smfmac_f32_32x32x32_bf16 v[64:79], v[118:121], v[0:7], v134\n"
            "v_exp_f32 v14, v14\n"
            "v_exp_f32 v15, v15\n"
            "v_exp_f32 v16, v16\n"
            "v_exp_f32 v17, v17\n"
            "v_smfmac_f32_32x32x32_bf16 v[64:79], v[122:125], v[8:15], v134\n"
            "v_exp_f32 v18, v18\n"
            "v_exp_f32 v19, v19\n"
            "v_exp_f32 v20, v20\n"
            "v_mfma_f32_32x32x16_bf16 v[32:47], v[108:111], v[102:105], v[32:47]\n"
            "ds_read_b128 v[108:111], v138 offset:5376\n"
            "v_exp_f32 v21, v21\n"
            "v_exp_f32 v22, v22\n"
            "v_exp_f32 v23, v23\n"
            "v_exp_f32 v24, v24\n"
            "v_mfma_f32_32x32x16_bf16 v[48:63], v[112:115], v[102:105], v[48:63]\n"
            "ds_read_b128 v[112:115], v138 offset:8064\n"
            "v_exp_f32 v25, v25\n"
            "v_exp_f32 v26, v26\n"
            "v_exp_f32 v27, v27\n"
            "v_exp_f32 v28, v28\n"
            "v_smfmac_f32_32x32x32_bf16 v[64:79], v[126:129], v[16:23], v134\n"
            "v_exp_f32 v29, v29\n"
            "v_exp_f32 v30, v30\n"
            "v_exp_f32 v31, v31\n"
            "v_exp_f32 v32, v32\n"
            "v_exp_f32 v33, v33\n"
            "v_exp_f32 v34, v34\n"
            "v_smfmac_f32_32x32x32_bf16 v[64:79], v[130:133], v[24:31], v134\n"
            "v_exp_f32 v35, v35\n"
            "v_exp_f32 v36, v36\n"
            "v_exp_f32 v37, v37\n"
            "s_waitcnt lgkmcnt(0)\n"
            "v_mfma_f32_32x32x4_2b_bf16 v[0:31], v[116:117], v[100:101], 0\n"
            "v_exp_f32 v38, v38\n"
            "v_exp_f32 v39, v39\n"
            "v_exp_f32 v40, v40\n"
            "v_exp_f32 v41, v41\n"
            "v_exp_f32 v42, v42\n"
            "v_exp_f32 v43, v43\n"
            "v_exp_f32 v44, v44\n"
            "v_exp_f32 v45, v45\n"
            "v_smfmac_f32_32x32x32_bf16 v[80:95], v[118:121], v[32:39], v134\n"
            "ds_read_b128 v[118:121], v140 offset:6400\n"
            "v_exp_f32 v46, v46\n"
            "v_exp_f32 v47, v47\n"
            "v_exp_f32 v48, v48\n"
            "v_exp_f32 v49, v49\n"
            "v_smfmac_f32_32x32x32_bf16 v[80:95], v[122:125], v[40:47], v134\n"
            "ds_read_b128 v[122:125], v140 offset:7104\n"
            "v_exp_f32 v50, v50\n"
            "v_exp_f32 v51, v51\n"
            "v_exp_f32 v52, v52\n"
            "v_mfma_f32_32x32x16_bf16 v[0:15], v[108:111], v[96:99], v[0:15]\n"
            "v_exp_f32 v53, v53\n"
            "v_exp_f32 v54, v54\n"
            "v_exp_f32 v55, v55\n"
            "v_exp_f32 v56, v56\n"
            "v_mfma_f32_32x32x16_bf16 v[16:31], v[112:115], v[96:99], v[16:31]\n"
            "v_exp_f32 v57, v57\n"
            "v_exp_f32 v58, v58\n"
            "v_exp_f32 v59, v59\n"
            "v_exp_f32 v60, v60\n"
            "v_smfmac_f32_32x32x32_bf16 v[80:95], v[126:129], v[48:55], v134\n"
            "ds_read_b128 v[126:129], v140 offset:9088\n"
            "v_exp_f32 v61, v61\n"
            "v_exp_f32 v62, v62\n"
            "v_exp_f32 v63, v63\n"
            "v_exp_f32 v0, v0\n"
            "v_exp_f32 v1, v1\n"
            "v_exp_f32 v2, v2\n"
            "v_smfmac_f32_32x32x32_bf16 v[80:95], v[130:133], v[56:63], v134\n"
            "ds_read_b128 v[130:133], v140 offset:9792\n"
            "v_exp_f32 v3, v3\n"
            "v_exp_f32 v4, v4\n"
            "v_exp_f32 v5, v5\n"
            "v_mfma_f32_32x32x4_2b_bf16 v[32:63], v[116:117], v[106:107], 0\n"
            "ds_read_b64 v[116:117], v139 offset:10752\n"
            "v_exp_f32 v6, v6\n"
            "v_exp_f32 v7, v7\n"
            "v_exp_f32 v8, v8\n"
            "v_exp_f32 v9, v9\n"
            "v_exp_f32 v10, v10\n"
            "v_exp_f32 v11, v11\n"
            "v_exp_f32 v12, v12\n"
            "v_exp_f32 v13, v13\n"
            "s_waitcnt lgkmcnt(1)\n"
            "v_smfmac_f32_32x32x32_bf16 v[64:79], v[118:121], v[0:7], v134\n"
            "v_exp_f32 v14, v14\n"
            "v_exp_f32 v15, v15\n"
            "v_exp_f32 v16, v16\n"
            "v_exp_f32 v17, v17\n"
            "v_smfmac_f32_32x32x32_bf16 v[64:79], v[122:125], v[8:15], v134\n"
            "v_exp_f32 v18, v18\n"
            "v_exp_f32 v19, v19\n"
            "v_exp_f32 v20, v20\n"
            "v_mfma_f32_32x32x16_bf16 v[32:47], v[108:111], v[102:105], v[32:47]\n"
            "ds_read_b128 v[108:111], v138 offset:10752\n"
            "v_exp_f32 v21, v21\n"
            "v_exp_f32 v22, v22\n"
            "v_exp_f32 v23, v23\n"
            "v_exp_f32 v24, v24\n"
            "v_mfma_f32_32x32x16_bf16 v[48:63], v[112:115], v[102:105], v[48:63]\n"
            "ds_read_b128 v[112:115], v138 offset:13440\n"
            "v_exp_f32 v25, v25\n"
            "v_exp_f32 v26, v26\n"
            "v_exp_f32 v27, v27\n"
            "v_exp_f32 v28, v28\n"
            "v_smfmac_f32_32x32x32_bf16 v[64:79], v[126:129], v[16:23], v134\n"
            "v_exp_f32 v29, v29\n"
            "v_exp_f32 v30, v30\n"
            "v_exp_f32 v31, v31\n"
            "v_exp_f32 v32, v32\n"
            "v_exp_f32 v33, v33\n"
            "v_exp_f32 v34, v34\n"
            "v_smfmac_f32_32x32x32_bf16 v[64:79], v[130:133], v[24:31], v134\n"
            "v_exp_f32 v35, v35\n"
            "v_exp_f32 v36, v36\n"
            "v_exp_f32 v37, v37\n"
            "s_waitcnt lgkmcnt(0)\n"
            "v_mfma_f32_32x32x4_2b_bf16 v[0:31], v[116:117], v[100:101], 0\n"
            "v_exp_f32 v38, v38\n"
            "v_exp_f32 v39, v39\n"
            "v_exp_f32 v40, v40\n"
            "v_exp_f32 v41, v41\n"
            "v_exp_f32 v42, v42\n"
            "v_exp_f32 v43, v43\n"
            "v_exp_f32 v44, v44\n"
            "v_exp_f32 v45, v45\n"
            "v_smfmac_f32_32x32x32_bf16 v[80:95], v[118:121], v[32:39], v134\n"
            "ds_read_b128 v[118:121], v140 offset:11776\n"
            "v_exp_f32 v46, v46\n"
            "v_exp_f32 v47, v47\n"
            "v_exp_f32 v48, v48\n"
            "v_exp_f32 v49, v49\n"
            "v_smfmac_f32_32x32x32_bf16 v[80:95], v[122:125], v[40:47], v134\n"
            "ds_read_b128 v[122:125], v140 offset:12480\n"
            "v_exp_f32 v50, v50\n"
            "v_exp_f32 v51, v51\n"
            "v_exp_f32 v52, v52\n"
            "v_mfma_f32_32x32x16_bf16 v[0:15], v[108:111], v[96:99], v[0:15]\n"
            "v_exp_f32 v53, v53\n"
            "v_exp_f32 v54, v54\n"
            "v_exp_f32 v55, v55\n"
            "v_exp_f32 v56, v56\n"
            "v_mfma_f32_32x32x16_bf16 v[16:31], v[112:115], v[96:99], v[16:31]\n"
            "v_exp_f32 v57, v57\n"
            "v_exp_f32 v58, v58\n"
            "v_exp_f32 v59, v59\n"
            "v_exp_f32 v60, v60\n"
            "v_smfmac_f32_32x32x32_bf16 v[80:95], v[126:129], v[48:55], v134\n"
            "ds_read_b128 v[126:129], v140 offset:14464\n"
            "v_exp_f32 v61, v61\n"
            "v_exp_f32 v62, v62\n"
            "v_exp_f32 v63, v63\n"
            "v_exp_f32 v0, v0\n"
            "v_exp_f32 v1, v1\n"
            "v_exp_f32 v2, v2\n"
            "v_smfmac_f32_32x32x32_bf16 v[80:95], v[130:133], v[56:63], v134\n"
            "ds_read_b128 v[130:133], v140 offset:15168\n"
            "v_exp_f32 v3, v3\n"
            "v_exp_f32 v4, v4\n"
            "v_exp_f32 v5, v5\n"
            "v_mfma_f32_32x32x4_2b_bf16 v[32:63], v[116:117], v[106:107], 0\n"
            "ds_read_b64 v[116:117], v139 offset:16128\n"
            "v_exp_f32 v6, v6\n"
            "v_exp_f32 v7, v7\n"
            "v_exp_f32 v8, v8\n"
            "v_exp_f32 v9, v9\n"
            "v_exp_f32 v10, v10\n"
            "v_exp_f32 v11, v11\n"
            "v_exp_f32 v12, v12\n"
            "v_exp_f32 v13, v13\n"
            "s_waitcnt lgkmcnt(1)\n"
            "v_smfmac_f32_32x32x32_bf16 v[64:79], v[118:121], v[0:7], v134\n"
            "v_exp_f32 v14, v14\n"
            "v_exp_f32 v15, v15\n"
            "v_exp_f32 v16, v16\n"
            "v_exp_f32 v17, v17\n"
            "v_smfmac_f32_32x32x32_bf16 v[64:79], v[122:125], v[8:15], v134\n"
            "v_exp_f32 v18, v18\n"
            "v_exp_f32 v19, v19\n"
            "v_exp_f32 v20, v20\n"
            "v_mfma_f32_32x32x16_bf16 v[32:47], v[108:111], v[102:105], v[32:47]\n"
            "ds_read_b128 v[108:111], v138 offset:16128\n"
            "v_exp_f32 v21, v21\n"
            "v_exp_f32 v22, v22\n"
            "v_exp_f32 v23, v23\n"
            "v_exp_f32 v24, v24\n"
            "v_mfma_f32_32x32x16_bf16 v[48:63], v[112:115], v[102:105], v[48:63]\n"
            "ds_read_b128 v[112:115], v138 offset:18816\n"
            "v_exp_f32 v25, v25\n"
            "v_exp_f32 v26, v26\n"
            "v_exp_f32 v27, v27\n"
            "v_exp_f32 v28, v28\n"
            "v_smfmac_f32_32x32x32_bf16 v[64:79], v[126:129], v[16:23], v134\n"
            "v_exp_f32 v29, v29\n"
            "v_exp_f32 v30, v30\n"
            "v_exp_f32 v31, v31\n"
            "v_exp_f32 v32, v32\n"
            "v_exp_f32 v33, v33\n"
            "v_exp_f32 v34, v34\n"
            "v_smfmac_f32_32x32x32_bf16 v[64:79], v[130:133], v[24:31], v134\n"
            "v_exp_f32 v35, v35\n"
            "v_exp_f32 v36, v36\n"
            "v_exp_f32 v37, v37\n"
            "s_waitcnt lgkmcnt(0)\n"
            "v_mfma_f32_32x32x4_2b_bf16 v[0:31], v[116:117], v[100:101], 0\n"
            "v_exp_f32 v38, v38\n"
            "v_exp_f32 v39, v39\n"
            "v_exp_f32 v40, v40\n"
            "v_exp_f32 v41, v41\n"
            "v_exp_f32 v42, v42\n"
            "v_exp_f32 v43, v43\n"
            "v_exp_f32 v44, v44\n"
            "v_exp_f32 v45, v45\n"
            "v_smfmac_f32_32x32x32_bf16 v[80:95], v[118:121], v[32:39], v134\n"
            "ds_read_b128 v[118:121], v140 offset:17152\n"
            "v_exp_f32 v46, v46\n"
            "v_exp_f32 v47, v47\n"
            "v_exp_f32 v48, v48\n"
            "v_exp_f32 v49, v49\n"
            "v_smfmac_f32_32x32x32_bf16 v[80:95], v[122:125], v[40:47], v134\n"
            "ds_read_b128 v[122:125], v140 offset:17856\n"
            "v_exp_f32 v50, v50\n"
            "v_exp_f32 v51, v51\n"
            "v_exp_f32 v52, v52\n"
            "v_mfma_f32_32x32x16_bf16 v[0:15], v[108:111], v[96:99], v[0:15]\n"
            "v_exp_f32 v53, v53\n"
            "v_exp_f32 v54, v54\n"
            "v_exp_f32 v55, v55\n"
            "v_exp_f32 v56, v56\n"
            "v_mfma_f32_32x32x16_bf16 v[16:31], v[112:115], v[96:99], v[16:31]\n"
            "v_exp_f32 v57, v57\n"
            "v_exp_f32 v58, v58\n"
            "v_exp_f32 v59, v59\n"
            "v_exp_f32 v60, v60\n"
            "v_smfmac_f32_32x32x32_bf16 v[80:95], v[126:129], v[48:55], v134\n"
            "ds_read_b128 v[126:129], v140 offset:19840\n"
            "v_exp_f32 v61, v61\n"
            "v_exp_f32 v62, v62\n"
            "v_exp_f32 v63, v63\n"
            "v_exp_f32 v0, v0\n"
            "v_exp_f32 v1, v1\n"
            "v_exp_f32 v2, v2\n"
            "v_smfmac_f32_32x32x32_bf16 v[80:95], v[130:133], v[56:63], v134\n"
            "ds_read_b128 v[130:133], v140 offset:20544\n"
            "v_exp_f32 v3, v3\n"
            "v_exp_f32 v4, v4\n"
            "v_exp_f32 v5, v5\n"
            "v_mfma_f32_32x32x4_2b_bf16 v[32:63], v[116:117], v[106:107], 0\n"
            "s_barrier\n"
            "ds_read_b64 v[116:117], v139 offset:21504\n"
            "v_exp_f32 v6, v6\n"
            "v_exp_f32 v7, v7\n"
            "v_exp_f32 v8, v8\n"
            "v_exp_f32 v9, v9\n"
            "v_exp_f32 v10, v10\n"
            "v_exp_f32 v11, v11\n"
            "v_exp_f32 v12, v12\n"
            "v_exp_f32 v13, v13\n"
            "s_waitcnt lgkmcnt(1)\n"
            "v_smfmac_f32_32x32x32_bf16 v[64:79], v[118:121], v[0:7], v134\n"
            "v_exp_f32 v14, v14\n"
            "v_exp_f32 v15, v15\n"
            "v_exp_f32 v16, v16\n"
            "v_exp_f32 v17, v17\n"
            "v_smfmac_f32_32x32x32_bf16 v[64:79], v[122:125], v[8:15], v134\n"
            "v_exp_f32 v18, v18\n"
            "v_exp_f32 v19, v19\n"
            "v_exp_f32 v20, v20\n"
            "v_mfma_f32_32x32x16_bf16 v[32:47], v[108:111], v[102:105], v[32:47]\n"
            "ds_read_b128 v[108:111], v138 offset:21504\n"
            "v_exp_f32 v21, v21\n"
            "v_exp_f32 v22, v22\n"
            "v_exp_f32 v23, v23\n"
            "v_exp_f32 v24, v24\n"
            "v_mfma_f32_32x32x16_bf16 v[48:63], v[112:115], v[102:105], v[48:63]\n"
            "ds_read_b128 v[112:115], v138 offset:24192\n"
            "v_exp_f32 v25, v25\n"
            "v_exp_f32 v26, v26\n"
            "v_exp_f32 v27, v27\n"
            "v_exp_f32 v28, v28\n"
            "v_smfmac_f32_32x32x32_bf16 v[64:79], v[126:129], v[16:23], v134\n"
            "v_exp_f32 v29, v29\n"
            "v_exp_f32 v30, v30\n"
            "v_exp_f32 v31, v31\n"
            "v_exp_f32 v32, v32\n"
            "v_exp_f32 v33, v33\n"
            "v_exp_f32 v34, v34\n"
            "v_smfmac_f32_32x32x32_bf16 v[64:79], v[130:133], v[24:31], v134\n"
            "v_exp_f32 v35, v35\n"
            "v_exp_f32 v36, v36\n"
            "v_exp_f32 v37, v37\n"
            "s_waitcnt lgkmcnt(0)\n"
            "v_mfma_f32_32x32x4_2b_bf16 v[0:31], v[116:117], v[100:101], 0\n"
            "v_exp_f32 v38, v38\n"
            "v_exp_f32 v39, v39\n"
            "v_exp_f32 v40, v40\n"
            "v_exp_f32 v41, v41\n"
            "v_exp_f32 v42, v42\n"
            "v_exp_f32 v43, v43\n"
            "v_exp_f32 v44, v44\n"
            "v_exp_f32 v45, v45\n"
            "v_smfmac_f32_32x32x32_bf16 v[80:95], v[118:121], v[32:39], v134\n"
            "ds_read_b128 v[118:121], v140 offset:22528\n"
            "v_exp_f32 v46, v46\n"
            "v_exp_f32 v47, v47\n"
            "v_exp_f32 v48, v48\n"
            "v_exp_f32 v49, v49\n"
            "v_smfmac_f32_32x32x32_bf16 v[80:95], v[122:125], v[40:47], v134\n"
            "ds_read_b128 v[122:125], v140 offset:23232\n"
            "v_exp_f32 v50, v50\n"
            "v_exp_f32 v51, v51\n"
            "v_exp_f32 v52, v52\n"
            "v_mfma_f32_32x32x16_bf16 v[0:15], v[108:111], v[96:99], v[0:15]\n"
            "v_exp_f32 v53, v53\n"
            "v_exp_f32 v54, v54\n"
            "v_exp_f32 v55, v55\n"
            "v_exp_f32 v56, v56\n"
            "v_mfma_f32_32x32x16_bf16 v[16:31], v[112:115], v[96:99], v[16:31]\n"
            "v_exp_f32 v57, v57\n"
            "v_exp_f32 v58, v58\n"
            "v_exp_f32 v59, v59\n"
            "v_exp_f32 v60, v60\n"
            "v_smfmac_f32_32x32x32_bf16 v[80:95], v[126:129], v[48:55], v134\n"
            "ds_read_b128 v[126:129], v140 offset:25216\n"
            "v_exp_f32 v61, v61\n"
            "v_exp_f32 v62, v62\n"
            "v_exp_f32 v63, v63\n"
            "s_cmp_eq_u32 s62, 2\n"
            "s_cbranch_scc1 4f\n"
            "v_exp_f32 v0, v0\n"
            "v_exp_f32 v1, v1\n"
            "v_exp_f32 v2, v2\n"
            "v_smfmac_f32_32x32x32_bf16 v[80:95], v[130:133], v[56:63], v134\n"
            "ds_read_b128 v[130:133], v140 offset:25920\n"
            "v_exp_f32 v3, v3\n"
            "v_exp_f32 v4, v4\n"
            "v_exp_f32 v5, v5\n"
            "v_mfma_f32_32x32x4_2b_bf16 v[32:63], v[116:117], v[106:107], 0\n"
            "ds_read_b64 v[116:117], v139 offset:26880\n"
            "v_exp_f32 v6, v6\n"
            "v_exp_f32 v7, v7\n"
            "v_exp_f32 v8, v8\n"
            "v_exp_f32 v9, v9\n"
            "v_exp_f32 v10, v10\n"
            "v_exp_f32 v11, v11\n"
            "v_exp_f32 v12, v12\n"
            "v_exp_f32 v13, v13\n"
            "s_waitcnt lgkmcnt(1)\n"
            "v_smfmac_f32_32x32x32_bf16 v[64:79], v[118:121], v[0:7], v134\n"
            "v_exp_f32 v14, v14\n"
            "v_exp_f32 v15, v15\n"
            "v_exp_f32 v16, v16\n"
            "v_exp_f32 v17, v17\n"
            "v_smfmac_f32_32x32x32_bf16 v[64:79], v[122:125], v[8:15], v134\n"
            "v_exp_f32 v18, v18\n"
            "v_exp_f32 v19, v19\n"
            "v_exp_f32 v20, v20\n"
            "v_mfma_f32_32x32x16_bf16 v[32:47], v[108:111], v[102:105], v[32:47]\n"
            "ds_read_b128 v[108:111], v138 offset:26880\n"
            "v_exp_f32 v21, v21\n"
            "v_exp_f32 v22, v22\n"
            "v_exp_f32 v23, v23\n"
            "v_exp_f32 v24, v24\n"
            "v_mfma_f32_32x32x16_bf16 v[48:63], v[112:115], v[102:105], v[48:63]\n"
            "ds_read_b128 v[112:115], v138 offset:29568\n"
            "v_exp_f32 v25, v25\n"
            "v_exp_f32 v26, v26\n"
            "v_exp_f32 v27, v27\n"
            "v_exp_f32 v28, v28\n"
            "v_smfmac_f32_32x32x32_bf16 v[64:79], v[126:129], v[16:23], v134\n"
            "v_exp_f32 v29, v29\n"
            "v_exp_f32 v30, v30\n"
            "v_exp_f32 v31, v31\n"
            "v_exp_f32 v32, v32\n"
            "v_exp_f32 v33, v33\n"
            "v_exp_f32 v34, v34\n"
            "v_smfmac_f32_32x32x32_bf16 v[64:79], v[130:133], v[24:31], v134\n"
            "v_exp_f32 v35, v35\n"
            "v_exp_f32 v36, v36\n"
            "v_exp_f32 v37, v37\n"
            "s_waitcnt lgkmcnt(0)\n"
            "v_mfma_f32_32x32x4_2b_bf16 v[0:31], v[116:117], v[100:101], 0\n"
            "v_exp_f32 v38, v38\n"
            "v_exp_f32 v39, v39\n"
            "v_exp_f32 v40, v40\n"
            "v_exp_f32 v41, v41\n"
            "v_exp_f32 v42, v42\n"
            "v_exp_f32 v43, v43\n"
            "v_exp_f32 v44, v44\n"
            "v_exp_f32 v45, v45\n"
            "v_smfmac_f32_32x32x32_bf16 v[80:95], v[118:121], v[32:39], v134\n"
            "ds_read_b128 v[118:121], v140 offset:27904\n"
            "v_exp_f32 v46, v46\n"
            "v_exp_f32 v47, v47\n"
            "v_exp_f32 v48, v48\n"
            "v_exp_f32 v49, v49\n"
            "v_smfmac_f32_32x32x32_bf16 v[80:95], v[122:125], v[40:47], v134\n"
            "ds_read_b128 v[122:125], v140 offset:28608\n"
            "v_exp_f32 v50, v50\n"
            "v_exp_f32 v51, v51\n"
            "v_exp_f32 v52, v52\n"
            "v_mfma_f32_32x32x16_bf16 v[0:15], v[108:111], v[96:99], v[0:15]\n"
            "v_exp_f32 v53, v53\n"
            "v_exp_f32 v54, v54\n"
            "v_exp_f32 v55, v55\n"
            "v_exp_f32 v56, v56\n"
            "v_mfma_f32_32x32x16_bf16 v[16:31], v[112:115], v[96:99], v[16:31]\n"
            "v_exp_f32 v57, v57\n"
            "v_exp_f32 v58, v58\n"
            "v_exp_f32 v59, v59\n"
            "v_exp_f32 v60, v60\n"
            "v_smfmac_f32_32x32x32_bf16 v[80:95], v[126:129], v[48:55], v134\n"
            "ds_read_b128 v[126:129], v140 offset:30592\n"
            "v_exp_f32 v61, v61\n"
            "v_exp_f32 v62, v62\n"
            "v_exp_f32 v63, v63\n"
            "v_exp_f32 v0, v0\n"
            "v_exp_f32 v1, v1\n"
            "v_exp_f32 v2, v2\n"
            "v_smfmac_f32_32x32x32_bf16 v[80:95], v[130:133], v[56:63], v134\n"
            "ds_read_b128 v[130:133], v140 offset:31296\n"
            "v_exp_f32 v3, v3\n"
            "v_exp_f32 v4, v4\n"
            "v_exp_f32 v5, v5\n"
            "v_mfma_f32_32x32x4_2b_bf16 v[32:63], v[116:117], v[106:107], 0\n"
            "ds_read_b64 v[116:117], v139 offset:32256\n"
            "v_exp_f32 v6, v6\n"
            "v_exp_f32 v7, v7\n"
            "v_exp_f32 v8, v8\n"
            "v_exp_f32 v9, v9\n"
            "v_exp_f32 v10, v10\n"
            "v_exp_f32 v11, v11\n"
            "v_exp_f32 v12, v12\n"
            "v_exp_f32 v13, v13\n"
            "s_waitcnt lgkmcnt(1)\n"
            "v_smfmac_f32_32x32x32_bf16 v[64:79], v[118:121], v[0:7], v134\n"
            "v_exp_f32 v14, v14\n"
            "v_exp_f32 v15, v15\n"
            "v_exp_f32 v16, v16\n"
            "v_exp_f32 v17, v17\n"
            "v_smfmac_f32_32x32x32_bf16 v[64:79], v[122:125], v[8:15], v134\n"
            "v_exp_f32 v18, v18\n"
            "v_exp_f32 v19, v19\n"
            "v_exp_f32 v20, v20\n"
            "v_mfma_f32_32x32x16_bf16 v[32:47], v[108:111], v[102:105], v[32:47]\n"
            "ds_read_b128 v[108:111], v138 offset:32256\n"
            "v_exp_f32 v21, v21\n"
            "v_exp_f32 v22, v22\n"
            "v_exp_f32 v23, v23\n"
            "v_exp_f32 v24, v24\n"
            "v_mfma_f32_32x32x16_bf16 v[48:63], v[112:115], v[102:105], v[48:63]\n"
            "ds_read_b128 v[112:115], v138 offset:34944\n"
            "v_exp_f32 v25, v25\n"
            "v_exp_f32 v26, v26\n"
            "v_exp_f32 v27, v27\n"
            "v_exp_f32 v28, v28\n"
            "v_smfmac_f32_32x32x32_bf16 v[64:79], v[126:129], v[16:23], v134\n"
            "v_exp_f32 v29, v29\n"
            "v_exp_f32 v30, v30\n"
            "v_exp_f32 v31, v31\n"
            "v_exp_f32 v32, v32\n"
            "v_exp_f32 v33, v33\n"
            "v_exp_f32 v34, v34\n"
            "v_smfmac_f32_32x32x32_bf16 v[64:79], v[130:133], v[24:31], v134\n"
            "v_exp_f32 v35, v35\n"
            "v_exp_f32 v36, v36\n"
            "v_exp_f32 v37, v37\n"
            "s_waitcnt lgkmcnt(0)\n"
            "v_mfma_f32_32x32x4_2b_bf16 v[0:31], v[116:117], v[100:101], 0\n"
            "v_exp_f32 v38, v38\n"
            "v_exp_f32 v39, v39\n"
            "v_exp_f32 v40, v40\n"
            "v_exp_f32 v41, v41\n"
            "v_exp_f32 v42, v42\n"
            "v_exp_f32 v43, v43\n"
            "v_exp_f32 v44, v44\n"
            "v_exp_f32 v45, v45\n"
            "v_smfmac_f32_32x32x32_bf16 v[80:95], v[118:121], v[32:39], v134\n"
            "ds_read_b128 v[118:121], v140 offset:33280\n"
            "v_exp_f32 v46, v46\n"
            "v_exp_f32 v47, v47\n"
            "v_exp_f32 v48, v48\n"
            "v_exp_f32 v49, v49\n"
            "v_smfmac_f32_32x32x32_bf16 v[80:95], v[122:125], v[40:47], v134\n"
            "ds_read_b128 v[122:125], v140 offset:33984\n"
            "v_exp_f32 v50, v50\n"
            "v_exp_f32 v51, v51\n"
            "v_exp_f32 v52, v52\n"
            "v_mfma_f32_32x32x16_bf16 v[0:15], v[108:111], v[96:99], v[0:15]\n"
            "v_exp_f32 v53, v53\n"
            "v_exp_f32 v54, v54\n"
            "v_exp_f32 v55, v55\n"
            "v_exp_f32 v56, v56\n"
            "v_mfma_f32_32x32x16_bf16 v[16:31], v[112:115], v[96:99], v[16:31]\n"
            "v_exp_f32 v57, v57\n"
            "v_exp_f32 v58, v58\n"
            "v_exp_f32 v59, v59\n"
            "v_exp_f32 v60, v60\n"
            "v_smfmac_f32_32x32x32_bf16 v[80:95], v[126:129], v[48:55], v134\n"
            "ds_read_b128 v[126:129], v140 offset:35968\n"
            "v_exp_f32 v61, v61\n"
            "v_exp_f32 v62, v62\n"
            "v_exp_f32 v63, v63\n"
            "v_exp_f32 v0, v0\n"
            "v_exp_f32 v1, v1\n"
            "v_exp_f32 v2, v2\n"
            "v_smfmac_f32_32x32x32_bf16 v[80:95], v[130:133], v[56:63], v134\n"
            "ds_read_b128 v[130:133], v140 offset:36672\n"
            "v_exp_f32 v3, v3\n"
            "v_exp_f32 v4, v4\n"
            "v_exp_f32 v5, v5\n"
            "v_mfma_f32_32x32x4_2b_bf16 v[32:63], v[116:117], v[106:107], 0\n"
            "ds_read_b64 v[116:117], v139 offset:37632\n"
            "v_exp_f32 v6, v6\n"
            "v_exp_f32 v7, v7\n"
            "v_exp_f32 v8, v8\n"
            "v_exp_f32 v9, v9\n"
            "v_exp_f32 v10, v10\n"
            "v_exp_f32 v11, v11\n"
            "v_exp_f32 v12, v12\n"
            "v_exp_f32 v13, v13\n"
            "s_waitcnt lgkmcnt(1)\n"
            "v_smfmac_f32_32x32x32_bf16 v[64:79], v[118:121], v[0:7], v134\n"
            "v_exp_f32 v14, v14\n"
            "v_exp_f32 v15, v15\n"
            "v_exp_f32 v16, v16\n"
            "v_exp_f32 v17, v17\n"
            "v_smfmac_f32_32x32x32_bf16 v[64:79], v[122:125], v[8:15], v134\n"
            "v_exp_f32 v18, v18\n"
            "v_exp_f32 v19, v19\n"
            "v_exp_f32 v20, v20\n"
            "v_mfma_f32_32x32x16_bf16 v[32:47], v[108:111], v[102:105], v[32:47]\n"
            "ds_read_b128 v[108:111], v138 offset:37632\n"
            "v_exp_f32 v21, v21\n"
            "v_exp_f32 v22, v22\n"
            "v_exp_f32 v23, v23\n"
            "v_exp_f32 v24, v24\n"
            "v_mfma_f32_32x32x16_bf16 v[48:63], v[112:115], v[102:105], v[48:63]\n"
            "ds_read_b128 v[112:115], v138 offset:40320\n"
            "v_exp_f32 v25, v25\n"
            "v_exp_f32 v26, v26\n"
            "v_exp_f32 v27, v27\n"
            "v_exp_f32 v28, v28\n"
            "v_smfmac_f32_32x32x32_bf16 v[64:79], v[126:129], v[16:23], v134\n"
            "v_exp_f32 v29, v29\n"
            "v_exp_f32 v30, v30\n"
            "v_exp_f32 v31, v31\n"
            "v_exp_f32 v32, v32\n"
            "v_exp_f32 v33, v33\n"
            "v_exp_f32 v34, v34\n"
            "v_smfmac_f32_32x32x32_bf16 v[64:79], v[130:133], v[24:31], v134\n"
            "v_exp_f32 v35, v35\n"
            "v_exp_f32 v36, v36\n"
            "v_exp_f32 v37, v37\n"
            "s_waitcnt lgkmcnt(0)\n"
            "v_mfma_f32_32x32x4_2b_bf16 v[0:31], v[116:117], v[100:101], 0\n"
            "v_exp_f32 v38, v38\n"
            "v_exp_f32 v39, v39\n"
            "v_exp_f32 v40, v40\n"
            "v_exp_f32 v41, v41\n"
            "v_exp_f32 v42, v42\n"
            "v_exp_f32 v43, v43\n"
            "v_exp_f32 v44, v44\n"
            "v_exp_f32 v45, v45\n"
            "v_smfmac_f32_32x32x32_bf16 v[80:95], v[118:121], v[32:39], v134\n"
            "ds_read_b128 v[118:121], v140 offset:38656\n"
            "v_exp_f32 v46, v46\n"
            "v_exp_f32 v47, v47\n"
            "v_exp_f32 v48, v48\n"
            "v_exp_f32 v49, v49\n"
            "v_smfmac_f32_32x32x32_bf16 v[80:95], v[122:125], v[40:47], v134\n"
            "ds_read_b128 v[122:125], v140 offset:39360\n"
            "v_exp_f32 v50, v50\n"
            "v_exp_f32 v51, v51\n"
            "v_exp_f32 v52, v52\n"
            "v_mfma_f32_32x32x16_bf16 v[0:15], v[108:111], v[96:99], v[0:15]\n"
            "v_exp_f32 v53, v53\n"
            "v_exp_f32 v54, v54\n"
            "v_exp_f32 v55, v55\n"
            "v_exp_f32 v56, v56\n"
            "v_mfma_f32_32x32x16_bf16 v[16:31], v[112:115], v[96:99], v[16:31]\n"
            "v_exp_f32 v57, v57\n"
            "v_exp_f32 v58, v58\n"
            "v_exp_f32 v59, v59\n"
            "v_exp_f32 v60, v60\n"
            "v_smfmac_f32_32x32x32_bf16 v[80:95], v[126:129], v[48:55], v134\n"
            "ds_read_b128 v[126:129], v140 offset:41344\n"
            "v_exp_f32 v61, v61\n"
            "v_exp_f32 v62, v62\n"
            "v_exp_f32 v63, v63\n"
            "v_exp_f32 v0, v0\n"
            "v_exp_f32 v1, v1\n"
            "v_exp_f32 v2, v2\n"
            "v_smfmac_f32_32x32x32_bf16 v[80:95], v[130:133], v[56:63], v134\n"
            "ds_read_b128 v[130:133], v140 offset:42048\n"
            "v_exp_f32 v3, v3\n"
            "v_exp_f32 v4, v4\n"
            "v_exp_f32 v5, v5\n"
            "v_mfma_f32_32x32x4_2b_bf16 v[32:63], v[116:117], v[106:107], 0\n"
            "s_barrier\n"
            "ds_read_b64 v[116:117], v139 offset:43008\n"
            "v_exp_f32 v6, v6\n"
            "v_exp_f32 v7, v7\n"
            "v_exp_f32 v8, v8\n"
            "v_exp_f32 v9, v9\n"
            "v_exp_f32 v10, v10\n"
            "v_exp_f32 v11, v11\n"
            "v_exp_f32 v12, v12\n"
            "v_exp_f32 v13, v13\n"
            "s_waitcnt lgkmcnt(1)\n"
            "v_smfmac_f32_32x32x32_bf16 v[64:79], v[118:121], v[0:7], v134\n"
            "v_exp_f32 v14, v14\n"
            "v_exp_f32 v15, v15\n"
            "v_exp_f32 v16, v16\n"
            "v_exp_f32 v17, v17\n"
            "v_smfmac_f32_32x32x32_bf16 v[64:79], v[122:125], v[8:15], v134\n"
            "v_exp_f32 v18, v18\n"
            "v_exp_f32 v19, v19\n"
            "v_exp_f32 v20, v20\n"
            "v_mfma_f32_32x32x16_bf16 v[32:47], v[108:111], v[102:105], v[32:47]\n"
            "ds_read_b128 v[108:111], v138 offset:43008\n"
            "v_exp_f32 v21, v21\n"
            "v_exp_f32 v22, v22\n"
            "v_exp_f32 v23, v23\n"
            "v_exp_f32 v24, v24\n"
            "v_mfma_f32_32x32x16_bf16 v[48:63], v[112:115], v[102:105], v[48:63]\n"
            "ds_read_b128 v[112:115], v138 offset:45696\n"
            "v_exp_f32 v25, v25\n"
            "v_exp_f32 v26, v26\n"
            "v_exp_f32 v27, v27\n"
            "v_exp_f32 v28, v28\n"
            "v_smfmac_f32_32x32x32_bf16 v[64:79], v[126:129], v[16:23], v134\n"
            "v_exp_f32 v29, v29\n"
            "v_exp_f32 v30, v30\n"
            "v_exp_f32 v31, v31\n"
            "v_exp_f32 v32, v32\n"
            "v_exp_f32 v33, v33\n"
            "v_exp_f32 v34, v34\n"
            "v_smfmac_f32_32x32x32_bf16 v[64:79], v[130:133], v[24:31], v134\n"
            "v_exp_f32 v35, v35\n"
            "v_exp_f32 v36, v36\n"
            "v_exp_f32 v37, v37\n"
            "s_waitcnt lgkmcnt(0)\n"
            "v_mfma_f32_32x32x4_2b_bf16 v[0:31], v[116:117], v[100:101], 0\n"
            "v_exp_f32 v38, v38\n"
            "v_exp_f32 v39, v39\n"
            "v_exp_f32 v40, v40\n"
            "v_exp_f32 v41, v41\n"
            "v_exp_f32 v42, v42\n"
            "v_exp_f32 v43, v43\n"
            "v_exp_f32 v44, v44\n"
            "v_exp_f32 v45, v45\n"
            "v_smfmac_f32_32x32x32_bf16 v[80:95], v[118:121], v[32:39], v134\n"
            "ds_read_b128 v[118:121], v140 offset:44032\n"
            "v_exp_f32 v46, v46\n"
            "v_exp_f32 v47, v47\n"
            "v_exp_f32 v48, v48\n"
            "v_exp_f32 v49, v49\n"
            "v_smfmac_f32_32x32x32_bf16 v[80:95], v[122:125], v[40:47], v134\n"
            "ds_read_b128 v[122:125], v140 offset:44736\n"
            "v_exp_f32 v50, v50\n"
            "v_exp_f32 v51, v51\n"
            "v_exp_f32 v52, v52\n"
            "v_mfma_f32_32x32x16_bf16 v[0:15], v[108:111], v[96:99], v[0:15]\n"
            "v_exp_f32 v53, v53\n"
            "v_exp_f32 v54, v54\n"
            "v_exp_f32 v55, v55\n"
            "v_exp_f32 v56, v56\n"
            "v_mfma_f32_32x32x16_bf16 v[16:31], v[112:115], v[96:99], v[16:31]\n"
            "v_exp_f32 v57, v57\n"
            "v_exp_f32 v58, v58\n"
            "v_exp_f32 v59, v59\n"
            "v_exp_f32 v60, v60\n"
            "v_smfmac_f32_32x32x32_bf16 v[80:95], v[126:129], v[48:55], v134\n"
            "ds_read_b128 v[126:129], v140 offset:46720\n"
            "v_exp_f32 v61, v61\n"
            "v_exp_f32 v62, v62\n"
            "v_exp_f32 v63, v63\n"
            "v_exp_f32 v0, v0\n"
            "v_exp_f32 v1, v1\n"
            "v_exp_f32 v2, v2\n"
            "v_smfmac_f32_32x32x32_bf16 v[80:95], v[130:133], v[56:63], v134\n"
            "ds_read_b128 v[130:133], v140 offset:47424\n"
            "v_exp_f32 v3, v3\n"
            "v_exp_f32 v4, v4\n"
            "v_exp_f32 v5, v5\n"
            "v_mfma_f32_32x32x4_2b_bf16 v[32:63], v[116:117], v[106:107], 0\n"
            "ds_read_b64 v[116:117], v139 offset:48384\n"
            "v_exp_f32 v6, v6\n"
            "v_exp_f32 v7, v7\n"
            "v_exp_f32 v8, v8\n"
            "v_exp_f32 v9, v9\n"
            "v_exp_f32 v10, v10\n"
            "v_exp_f32 v11, v11\n"
            "v_exp_f32 v12, v12\n"
            "v_exp_f32 v13, v13\n"
            "s_waitcnt lgkmcnt(1)\n"
            "v_smfmac_f32_32x32x32_bf16 v[64:79], v[118:121], v[0:7], v134\n"
            "v_exp_f32 v14, v14\n"
            "v_exp_f32 v15, v15\n"
            "v_exp_f32 v16, v16\n"
            "v_exp_f32 v17, v17\n"
            "v_smfmac_f32_32x32x32_bf16 v[64:79], v[122:125], v[8:15], v134\n"
            "v_exp_f32 v18, v18\n"
            "v_exp_f32 v19, v19\n"
            "v_exp_f32 v20, v20\n"
            "v_mfma_f32_32x32x16_bf16 v[32:47], v[108:111], v[102:105], v[32:47]\n"
            "ds_read_b128 v[108:111], v138 offset:48384\n"
            "v_exp_f32 v21, v21\n"
            "v_exp_f32 v22, v22\n"
            "v_exp_f32 v23, v23\n"
            "v_exp_f32 v24, v24\n"
            "v_mfma_f32_32x32x16_bf16 v[48:63], v[112:115], v[102:105], v[48:63]\n"
            "ds_read_b128 v[112:115], v138 offset:51072\n"
            "v_exp_f32 v25, v25\n"
            "v_exp_f32 v26, v26\n"
            "v_exp_f32 v27, v27\n"
            "v_exp_f32 v28, v28\n"
            "v_smfmac_f32_32x32x32_bf16 v[64:79], v[126:129], v[16:23], v134\n"
            "v_exp_f32 v29, v29\n"
            "v_exp_f32 v30, v30\n"
            "v_exp_f32 v31, v31\n"
            "v_exp_f32 v32, v32\n"
            "v_exp_f32 v33, v33\n"
            "v_exp_f32 v34, v34\n"
            "v_smfmac_f32_32x32x32_bf16 v[64:79], v[130:133], v[24:31], v134\n"
            "v_exp_f32 v35, v35\n"
            "v_exp_f32 v36, v36\n"
            "v_exp_f32 v37, v37\n"
            "s_waitcnt lgkmcnt(0)\n"
            "v_mfma_f32_32x32x4_2b_bf16 v[0:31], v[116:117], v[100:101], 0\n"
            "v_exp_f32 v38, v38\n"
            "v_exp_f32 v39, v39\n"
            "v_exp_f32 v40, v40\n"
            "v_exp_f32 v41, v41\n"
            "v_exp_f32 v42, v42\n"
            "v_exp_f32 v43, v43\n"
            "v_exp_f32 v44, v44\n"
            "v_exp_f32 v45, v45\n"
            "v_smfmac_f32_32x32x32_bf16 v[80:95], v[118:121], v[32:39], v134\n"
            "ds_read_b128 v[118:121], v140 offset:49408\n"
            "v_exp_f32 v46, v46\n"
            "v_exp_f32 v47, v47\n"
            "v_exp_f32 v48, v48\n"
            "v_exp_f32 v49, v49\n"
            "v_smfmac_f32_32x32x32_bf16 v[80:95], v[122:125], v[40:47], v134\n"
            "ds_read_b128 v[122:125], v140 offset:50112\n"
            "v_exp_f32 v50, v50\n"
            "v_exp_f32 v51, v51\n"
            "v_exp_f32 v52, v52\n"
            "v_mfma_f32_32x32x16_bf16 v[0:15], v[108:111], v[96:99], v[0:15]\n"
            "v_exp_f32 v53, v53\n"
            "v_exp_f32 v54, v54\n"
            "v_exp_f32 v55, v55\n"
            "v_exp_f32 v56, v56\n"
            "v_mfma_f32_32x32x16_bf16 v[16:31], v[112:115], v[96:99], v[16:31]\n"
            "v_exp_f32 v57, v57\n"
            "v_exp_f32 v58, v58\n"
            "v_exp_f32 v59, v59\n"
            "v_exp_f32 v60, v60\n"
            "v_smfmac_f32_32x32x32_bf16 v[80:95], v[126:129], v[48:55], v134\n"
            "ds_read_b128 v[126:129], v140 offset:52096\n"
            "v_exp_f32 v61, v61\n"
            "v_exp_f32 v62, v62\n"
            "v_exp_f32 v63, v63\n"
            "v_exp_f32 v0, v0\n"
            "v_exp_f32 v1, v1\n"
            "v_exp_f32 v2, v2\n"
            "v_smfmac_f32_32x32x32_bf16 v[80:95], v[130:133], v[56:63], v134\n"
            "ds_read_b128 v[130:133], v140 offset:52800\n"
            "v_exp_f32 v3, v3\n"
            "v_exp_f32 v4, v4\n"
            "v_exp_f32 v5, v5\n"
            "v_mfma_f32_32x32x4_2b_bf16 v[32:63], v[116:117], v[106:107], 0\n"
            "ds_read_b64 v[116:117], v139 offset:53760\n"
            "v_exp_f32 v6, v6\n"
            "v_exp_f32 v7, v7\n"
            "v_exp_f32 v8, v8\n"
            "v_exp_f32 v9, v9\n"
            "v_exp_f32 v10, v10\n"
            "v_exp_f32 v11, v11\n"
            "v_exp_f32 v12, v12\n"
            "v_exp_f32 v13, v13\n"
            "s_waitcnt lgkmcnt(1)\n"
            "v_smfmac_f32_32x32x32_bf16 v[64:79], v[118:121], v[0:7], v134\n"
            "v_exp_f32 v14, v14\n"
            "v_exp_f32 v15, v15\n"
            "v_exp_f32 v16, v16\n"
            "v_exp_f32 v17, v17\n"
            "v_smfmac_f32_32x32x32_bf16 v[64:79], v[122:125], v[8:15], v134\n"
            "v_exp_f32 v18, v18\n"
            "v_exp_f32 v19, v19\n"
            "v_exp_f32 v20, v20\n"
            "v_mfma_f32_32x32x16_bf16 v[32:47], v[108:111], v[102:105], v[32:47]\n"
            "ds_read_b128 v[108:111], v138 offset:53760\n"
            "v_exp_f32 v21, v21\n"
            "v_exp_f32 v22, v22\n"
            "v_exp_f32 v23, v23\n"
            "v_exp_f32 v24, v24\n"
            "v_mfma_f32_32x32x16_bf16 v[48:63], v[112:115], v[102:105], v[48:63]\n"
            "ds_read_b128 v[112:115], v138 offset:56448\n"
            "v_exp_f32 v25, v25\n"
            "v_exp_f32 v26, v26\n"
            "v_exp_f32 v27, v27\n"
            "v_exp_f32 v28, v28\n"
            "v_smfmac_f32_32x32x32_bf16 v[64:79], v[126:129], v[16:23], v134\n"
            "v_exp_f32 v29, v29\n"
            "v_exp_f32 v30, v30\n"
            "v_exp_f32 v31, v31\n"
            "v_exp_f32 v32, v32\n"
            "v_exp_f32 v33, v33\n"
            "v_exp_f32 v34, v34\n"
            "v_smfmac_f32_32x32x32_bf16 v[64:79], v[130:133], v[24:31], v134\n"
            "v_exp_f32 v35, v35\n"
            "v_exp_f32 v36, v36\n"
            "v_exp_f32 v37, v37\n"
            "s_waitcnt lgkmcnt(0)\n"
            "v_mfma_f32_32x32x4_2b_bf16 v[0:31], v[116:117], v[100:101], 0\n"
            "v_exp_f32 v38, v38\n"
            "v_exp_f32 v39, v39\n"
            "v_exp_f32 v40, v40\n"
            "v_exp_f32 v41, v41\n"
            "v_exp_f32 v42, v42\n"
            "v_exp_f32 v43, v43\n"
            "v_exp_f32 v44, v44\n"
            "v_exp_f32 v45, v45\n"
            "v_smfmac_f32_32x32x32_bf16 v[80:95], v[118:121], v[32:39], v134\n"
            "ds_read_b128 v[118:121], v140 offset:54784\n"
            "v_exp_f32 v46, v46\n"
            "v_exp_f32 v47, v47\n"
            "v_exp_f32 v48, v48\n"
            "v_exp_f32 v49, v49\n"
            "v_smfmac_f32_32x32x32_bf16 v[80:95], v[122:125], v[40:47], v134\n"
            "ds_read_b128 v[122:125], v140 offset:55488\n"
            "v_exp_f32 v50, v50\n"
            "v_exp_f32 v51, v51\n"
            "v_exp_f32 v52, v52\n"
            "v_mfma_f32_32x32x16_bf16 v[0:15], v[108:111], v[96:99], v[0:15]\n"
            "v_exp_f32 v53, v53\n"
            "v_exp_f32 v54, v54\n"
            "v_exp_f32 v55, v55\n"
            "v_exp_f32 v56, v56\n"
            "v_mfma_f32_32x32x16_bf16 v[16:31], v[112:115], v[96:99], v[16:31]\n"
            "v_exp_f32 v57, v57\n"
            "v_exp_f32 v58, v58\n"
            "v_exp_f32 v59, v59\n"
            "v_exp_f32 v60, v60\n"
            "v_smfmac_f32_32x32x32_bf16 v[80:95], v[126:129], v[48:55], v134\n"
            "ds_read_b128 v[126:129], v140 offset:57472\n"
            "v_exp_f32 v61, v61\n"
            "v_exp_f32 v62, v62\n"
            "v_exp_f32 v63, v63\n"
            "v_exp_f32 v0, v0\n"
            "v_exp_f32 v1, v1\n"
            "v_exp_f32 v2, v2\n"
            "v_smfmac_f32_32x32x32_bf16 v[80:95], v[130:133], v[56:63], v134\n"
            "ds_read_b128 v[130:133], v140 offset:58176\n"
            "v_exp_f32 v3, v3\n"
            "v_exp_f32 v4, v4\n"
            "v_exp_f32 v5, v5\n"
            "v_mfma_f32_32x32x4_2b_bf16 v[32:63], v[116:117], v[106:107], 0\n"
            "ds_read_b64 v[116:117], v139 offset:59136\n"
            "v_exp_f32 v6, v6\n"
            "v_exp_f32 v7, v7\n"
            "v_exp_f32 v8, v8\n"
            "v_exp_f32 v9, v9\n"
            "v_exp_f32 v10, v10\n"
            "v_exp_f32 v11, v11\n"
            "v_exp_f32 v12, v12\n"
            "v_exp_f32 v13, v13\n"
            "s_waitcnt lgkmcnt(1)\n"
            "v_smfmac_f32_32x32x32_bf16 v[64:79], v[118:121], v[0:7], v134\n"
            "v_exp_f32 v14, v14\n"
            "v_exp_f32 v15, v15\n"
            "v_exp_f32 v16, v16\n"
            "v_exp_f32 v17, v17\n"
            "v_smfmac_f32_32x32x32_bf16 v[64:79], v[122:125], v[8:15], v134\n"
            "v_exp_f32 v18, v18\n"
            "v_exp_f32 v19, v19\n"
            "v_exp_f32 v20, v20\n"
            "v_mfma_f32_32x32x16_bf16 v[32:47], v[108:111], v[102:105], v[32:47]\n"
            "ds_read_b128 v[108:111], v138 offset:59136\n"
            "v_exp_f32 v21, v21\n"
            "v_exp_f32 v22, v22\n"
            "v_exp_f32 v23, v23\n"
            "v_exp_f32 v24, v24\n"
            "v_mfma_f32_32x32x16_bf16 v[48:63], v[112:115], v[102:105], v[48:63]\n"
            "ds_read_b128 v[112:115], v138 offset:61824\n"
            "v_exp_f32 v25, v25\n"
            "v_exp_f32 v26, v26\n"
            "v_exp_f32 v27, v27\n"
            "v_exp_f32 v28, v28\n"
            "v_smfmac_f32_32x32x32_bf16 v[64:79], v[126:129], v[16:23], v134\n"
            "v_exp_f32 v29, v29\n"
            "v_exp_f32 v30, v30\n"
            "v_exp_f32 v31, v31\n"
            "v_exp_f32 v32, v32\n"
            "v_exp_f32 v33, v33\n"
            "v_exp_f32 v34, v34\n"
            "v_smfmac_f32_32x32x32_bf16 v[64:79], v[130:133], v[24:31], v134\n"
            "v_exp_f32 v35, v35\n"
            "v_exp_f32 v36, v36\n"
            "v_exp_f32 v37, v37\n"
            "s_waitcnt lgkmcnt(0)\n"
            "v_mfma_f32_32x32x4_2b_bf16 v[0:31], v[116:117], v[100:101], 0\n"
            "v_exp_f32 v38, v38\n"
            "v_exp_f32 v39, v39\n"
            "v_exp_f32 v40, v40\n"
            "v_exp_f32 v41, v41\n"
            "v_exp_f32 v42, v42\n"
            "v_exp_f32 v43, v43\n"
            "v_exp_f32 v44, v44\n"
            "v_exp_f32 v45, v45\n"
            "v_smfmac_f32_32x32x32_bf16 v[80:95], v[118:121], v[32:39], v134\n"
            "ds_read_b128 v[118:121], v140 offset:60160\n"
            "v_exp_f32 v46, v46\n"
            "v_exp_f32 v47, v47\n"
            "v_exp_f32 v48, v48\n"
            "v_exp_f32 v49, v49\n"
            "v_smfmac_f32_32x32x32_bf16 v[80:95], v[122:125], v[40:47], v134\n"
            "ds_read_b128 v[122:125], v140 offset:60864\n"
            "v_exp_f32 v50, v50\n"
            "v_exp_f32 v51, v51\n"
            "v_exp_f32 v52, v52\n"
            "v_mfma_f32_32x32x16_bf16 v[0:15], v[108:111], v[96:99], v[0:15]\n"
            "v_exp_f32 v53, v53\n"
            "v_exp_f32 v54, v54\n"
            "v_exp_f32 v55, v55\n"
            "v_exp_f32 v56, v56\n"
            "v_mfma_f32_32x32x16_bf16 v[16:31], v[112:115], v[96:99], v[16:31]\n"
            "v_exp_f32 v57, v57\n"
            "v_exp_f32 v58, v58\n"
            "v_exp_f32 v59, v59\n"
            "v_exp_f32 v60, v60\n"
            "v_smfmac_f32_32x32x32_bf16 v[80:95], v[126:129], v[48:55], v134\n"
            "ds_read_b128 v[126:129], v140 offset:62848\n"
            "v_exp_f32 v61, v61\n"
            "v_exp_f32 v62, v62\n"
            "v_exp_f32 v63, v63\n"
            "v_exp_f32 v0, v0\n"
            "v_exp_f32 v1, v1\n"
            "v_exp_f32 v2, v2\n"
            "v_smfmac_f32_32x32x32_bf16 v[80:95], v[130:133], v[56:63], v134\n"
            "ds_read_b128 v[130:133], v140 offset:63552\n"
            "v_exp_f32 v3, v3\n"
            "v_exp_f32 v4, v4\n"
            "v_exp_f32 v5, v5\n"
            "v_mfma_f32_32x32x4_2b_bf16 v[32:63], v[116:117], v[106:107], 0\n"
            "s_barrier\n"
            "ds_read_b64 v[116:117], v139 offset:0\n"
            "v_exp_f32 v6, v6\n"
            "v_exp_f32 v7, v7\n"
            "v_exp_f32 v8, v8\n"
            "v_exp_f32 v9, v9\n"
            "v_exp_f32 v10, v10\n"
            "v_exp_f32 v11, v11\n"
            "v_exp_f32 v12, v12\n"
            "v_exp_f32 v13, v13\n"
            "s_waitcnt lgkmcnt(1)\n"
            "v_smfmac_f32_32x32x32_bf16 v[64:79], v[118:121], v[0:7], v134\n"
            "v_exp_f32 v14, v14\n"
            "v_exp_f32 v15, v15\n"
            "v_exp_f32 v16, v16\n"
            "v_exp_f32 v17, v17\n"
            "v_smfmac_f32_32x32x32_bf16 v[64:79], v[122:125], v[8:15], v134\n"
            "v_exp_f32 v18, v18\n"
            "v_exp_f32 v19, v19\n"
            "v_exp_f32 v20, v20\n"
            "v_mfma_f32_32x32x16_bf16 v[32:47], v[108:111], v[102:105], v[32:47]\n"
            "ds_read_b128 v[108:111], v138 offset:0\n"
            "v_exp_f32 v21, v21\n"
            "v_exp_f32 v22, v22\n"
            "v_exp_f32 v23, v23\n"
            "v_exp_f32 v24, v24\n"
            "v_mfma_f32_32x32x16_bf16 v[48:63], v[112:115], v[102:105], v[48:63]\n"
            "ds_read_b128 v[112:115], v138 offset:2688\n"
            "v_exp_f32 v25, v25\n"
            "v_exp_f32 v26, v26\n"
            "v_exp_f32 v27, v27\n"
            "v_exp_f32 v28, v28\n"
            "v_smfmac_f32_32x32x32_bf16 v[64:79], v[126:129], v[16:23], v134\n"
            "v_exp_f32 v29, v29\n"
            "v_exp_f32 v30, v30\n"
            "v_exp_f32 v31, v31\n"
            "v_exp_f32 v32, v32\n"
            "v_exp_f32 v33, v33\n"
            "v_exp_f32 v34, v34\n"
            "v_smfmac_f32_32x32x32_bf16 v[64:79], v[130:133], v[24:31], v134\n"
            "v_exp_f32 v35, v35\n"
            "v_exp_f32 v36, v36\n"
            "v_exp_f32 v37, v37\n"
            "s_waitcnt lgkmcnt(0)\n"
            "v_mfma_f32_32x32x4_2b_bf16 v[0:31], v[116:117], v[100:101], 0\n"
            "v_exp_f32 v38, v38\n"
            "v_exp_f32 v39, v39\n"
            "v_exp_f32 v40, v40\n"
            "v_exp_f32 v41, v41\n"
            "v_exp_f32 v42, v42\n"
            "v_exp_f32 v43, v43\n"
            "v_exp_f32 v44, v44\n"
            "v_exp_f32 v45, v45\n"
            "v_smfmac_f32_32x32x32_bf16 v[80:95], v[118:121], v[32:39], v134\n"
            "ds_read_b128 v[118:121], v140 offset:1024\n"
            "v_exp_f32 v46, v46\n"
            "v_exp_f32 v47, v47\n"
            "v_exp_f32 v48, v48\n"
            "v_exp_f32 v49, v49\n"
            "v_smfmac_f32_32x32x32_bf16 v[80:95], v[122:125], v[40:47], v134\n"
            "ds_read_b128 v[122:125], v140 offset:1728\n"
            "v_exp_f32 v50, v50\n"
            "v_exp_f32 v51, v51\n"
            "v_exp_f32 v52, v52\n"
            "v_mfma_f32_32x32x16_bf16 v[0:15], v[108:111], v[96:99], v[0:15]\n"
            "v_exp_f32 v53, v53\n"
            "v_exp_f32 v54, v54\n"
            "v_exp_f32 v55, v55\n"
            "v_exp_f32 v56, v56\n"
            "v_mfma_f32_32x32x16_bf16 v[16:31], v[112:115], v[96:99], v[16:31]\n"
            "v_exp_f32 v57, v57\n"
            "v_exp_f32 v58, v58\n"
            "v_exp_f32 v59, v59\n"
            "v_exp_f32 v60, v60\n"
            "v_smfmac_f32_32x32x32_bf16 v[80:95], v[126:129], v[48:55], v134\n"
            "ds_read_b128 v[126:129], v140 offset:3712\n"
            "v_exp_f32 v61, v61\n"
            "v_exp_f32 v62, v62\n"
            "v_exp_f32 v63, v63\n"
            "s_add_u32 s62, s62, 1\n"
            "s_branch 2b\n"
            "4:\n"
            "s_add_u32 s64, %[wlo], 24576\n"
            "s_addc_u32 s65, %[whi], 0\n"
            "v_exp_f32 v0, v0\n"
            "v_exp_f32 v1, v1\n"
            "global_load_dwordx4 v[142:145], v141, s[64:65] offset:0 sc1\n"
            "v_exp_f32 v2, v2\n"
            "v_smfmac_f32_32x32x32_bf16 v[80:95], v[130:133], v[56:63], v134\n"
            "ds_read_b128 v[130:133], v140 offset:25920\n"
            "v_exp_f32 v3, v3\n"
            "v_exp_f32 v4, v4\n"
            "v_exp_f32 v5, v5\n"
            "v_mfma_f32_32x32x4_2b_bf16 v[32:63], v[116:117], v[106:107], 0\n"
            "ds_read_b64 v[116:117], v139 offset:26880\n"
            "v_exp_f32 v6, v6\n"
            "v_exp_f32 v7, v7\n"
            "v_exp_f32 v8, v8\n"
            "v_exp_f32 v9, v9\n"
            "global_load_dwordx4 v[146:149], v141, s[64:65] offset:1024 sc1\n"
            "v_exp_f32 v10, v10\n"
            "v_exp_f32 v11, v11\n"
            "v_exp_f32 v12, v12\n"
            "v_exp_f32 v13, v13\n"
            "s_waitcnt lgkmcnt(1)\n"
            "v_smfmac_f32_32x32x32_bf16 v[64:79], v[118:121], v[0:7], v134\n"
            "v_exp_f32 v14, v14\n"
            "v_exp_f32 v15, v15\n"
            "v_exp_f32 v16, v16\n"
            "v_exp_f32 v17, v17\n"
            "v_smfmac_f32_32x32x32_bf16 v[64:79], v[122:125], v[8:15], v134\n"
            "v_exp_f32 v18, v18\n"
            "v_exp_f32 v19, v19\n"
            "global_load_dwordx4 v[150:153], v141, s[64:65] offset:2048 sc1\n"
            "v_exp_f32 v20, v20\n"
            "v_mfma_f32_32x32x16_bf16 v[32:47], v[108:111], v[102:105], v[32:47]\n"
            "ds_read_b128 v[108:111], v138 offset:26880\n"
            "v_exp_f32 v21, v21\n"
            "v_exp_f32 v22, v22\n"
            "v_exp_f32 v23, v23\n"
            "v_exp_f32 v24, v24\n"
            "v_mfma_f32_32x32x16_bf16 v[48:63], v[112:115], v[102:105], v[48:63]\n"
            "ds_read_b128 v[112:115], v138 offset:29568\n"
            "v_exp_f32 v25, v25\n"
            "v_exp_f32 v26, v26\n"
            "v_exp_f32 v27, v27\n"
            "global_load_dwordx4 v[154:157], v141, s[64:65] offset:3072 sc1\n"
            "v_exp_f32 v28, v28\n"
            "v_smfmac_f32_32x32x32_bf16 v[64:79], v[126:129], v[16:23], v134\n"
            "v_exp_f32 v29, v29\n"
            "v_exp_f32 v30, v30\n"
            "v_exp_f32 v31, v31\n"
            "s_add_u32 s64, %[wlo], 28672\n"
            "s_addc_u32 s65, %[whi], 0\n"
            "v_exp_f32 v32, v32\n"
            "v_exp_f32 v33, v33\n"
            "global_load_dwordx4 v[158:161], v141, s[64:65] offset:0 sc1\n"
            "v_exp_f32 v34, v34\n"
            "v_smfmac_f32_32x32x32_bf16 v[64:79], v[130:133], v[24:31], v134\n"
            "v_exp_f32 v35, v35\n"
            "v_exp_f32 v36, v36\n"
            "v_exp_f32 v37, v37\n"
            "s_waitcnt lgkmcnt(0)\n"
            "v_mfma_f32_32x32x4_2b_bf16 v[0:31], v[116:117], v[100:101], 0\n"
            "v_exp_f32 v38, v38\n"
            "v_exp_f32 v39, v39\n"
            "v_exp_f32 v40, v40\n"
            "v_exp_f32 v41, v41\n"
            "global_load_dwordx4 v[162:165], v141, s[64:65] offset:1024 sc1\n"
            "v_exp_f32 v42, v42\n"
            "v_exp_f32 v43, v43\n"
            "v_exp_f32 v44, v44\n"
            "v_exp_f32 v45, v45\n"
            "v_smfmac_f32_32x32x32_bf16 v[80:95], v[118:121], v[32:39], v134\n"
            "ds_read_b128 v[118:121], v140 offset:27904\n"
            "v_exp_f32 v46, v46\n"
            "v_exp_f32 v47, v47\n"
            "v_exp_f32 v48, v48\n"
            "v_exp_f32 v49, v49\n"
            "v_smfmac_f32_32x32x32_bf16 v[80:95], v[122:125], v[40:47], v134\n"
            "ds_read_b128 v[122:125], v140 offset:28608\n"
            "v_exp_f32 v50, v50\n"
            "v_exp_f32 v51, v51\n"
            "global_load_dwordx4 v[166:169], v141, s[64:65] offset:2048 sc1\n"
            "v_exp_f32 v52, v52\n"
            "v_mfma_f32_32x32x16_bf16 v[0:15], v[108:111], v[96:99], v[0:15]\n"
            "v_exp_f32 v53, v53\n"
            "v_exp_f32 v54, v54\n"
            "v_exp_f32 v55, v55\n"
            "v_exp_f32 v56, v56\n"
            "v_mfma_f32_32x32x16_bf16 v[16:31], v[112:115], v[96:99], v[16:31]\n"
            "v_exp_f32 v57, v57\n"
            "v_exp_f32 v58, v58\n"
            "v_exp_f32 v59, v59\n"
            "global_load_dwordx4 v[170:173], v141, s[64:65] offset:3072 sc1\n"
            "v_exp_f32 v60, v60\n"
            "v_smfmac_f32_32x32x32_bf16 v[80:95], v[126:129], v[48:55], v134\n"
            "ds_read_b128 v[126:129], v140 offset:30592\n"
            "v_exp_f32 v61, v61\n"
            "v_exp_f32 v62, v62\n"
            "v_exp_f32 v63, v63\n"
            "s_add_u32 s64, %[wlo], 57344\n"
            "s_addc_u32 s65, %[whi], 0\n"
            "v_exp_f32 v0, v0\n"
            "v_exp_f32 v1, v1\n"
            "global_load_dwordx4 v[174:177], v141, s[64:65] offset:0 sc1\n"
            "v_exp_f32 v2, v2\n"
            "v_smfmac_f32_32x32x32_bf16 v[80:95], v[130:133], v[56:63], v134\n"
            "ds_read_b128 v[130:133], v140 offset:31296\n"
            "v_exp_f32 v3, v3\n"
            "v_exp_f32 v4, v4\n"
            "v_exp_f32 v5, v5\n"
            "v_mfma_f32_32x32x4_2b_bf16 v[32:63], v[116:117], v[106:107], 0\n"
            "ds_read_b64 v[116:117], v139 offset:32256\n"
            "v_exp_f32 v6, v6\n"
            "v_exp_f32 v7, v7\n"
            "v_exp_f32 v8, v8\n"
            "v_exp_f32 v9, v9\n"
            "global_load_dwordx4 v[178:181], v141, s[64:65] offset:1024 sc1\n"
            "v_exp_f32 v10, v10\n"
            "v_exp_f32 v11, v11\n"
            "v_exp_f32 v12, v12\n"
            "v_exp_f32 v13, v13\n"
            "s_waitcnt lgkmcnt(1)\n"
            "v_smfmac_f32_32x32x32_bf16 v[64:79], v[118:121], v[0:7], v134\n"
            "v_exp_f32 v14, v14\n"
            "v_exp_f32 v15, v15\n"
            "v_exp_f32 v16, v16\n"
            "v_exp_f32 v17, v17\n"
            "v_smfmac_f32_32x32x32_bf16 v[64:79], v[122:125], v[8:15], v134\n"
            "v_exp_f32 v18, v18\n"
            "v_exp_f32 v19, v19\n"
            "global_load_dwordx4 v[182:185], v141, s[64:65] offset:2048 sc1\n"
            "v_exp_f32 v20, v20\n"
            "v_mfma_f32_32x32x16_bf16 v[32:47], v[108:111], v[102:105], v[32:47]\n"
            "ds_read_b128 v[108:111], v138 offset:32256\n"
            "v_exp_f32 v21, v21\n"
            "v_exp_f32 v22, v22\n"
            "v_exp_f32 v23, v23\n"
            "v_exp_f32 v24, v24\n"
            "v_mfma_f32_32x32x16_bf16 v[48:63], v[112:115], v[102:105], v[48:63]\n"
            "ds_read_b128 v[112:115], v138 offset:34944\n"
            "v_exp_f32 v25, v25\n"
            "v_exp_f32 v26, v26\n"
            "v_exp_f32 v27, v27\n"
            "global_load_dwordx4 v[186:189], v141, s[64:65] offset:3072 sc1\n"
            "v_exp_f32 v28, v28\n"
            "v_smfmac_f32_32x32x32_bf16 v[64:79], v[126:129], v[16:23], v134\n"
            "v_exp_f32 v29, v29\n"
            "v_exp_f32 v30, v30\n"
            "v_exp_f32 v31, v31\n"
            "s_add_u32 s64, %[wlo], 61440\n"
            "s_addc_u32 s65, %[whi], 0\n"
            "v_exp_f32 v32, v32\n"
            "v_exp_f32 v33, v33\n"
            "global_load_dwordx4 v[190:193], v141, s[64:65] offset:0 sc1\n"
            "v_exp_f32 v34, v34\n"
            "v_smfmac_f32_32x32x32_bf16 v[64:79], v[130:133], v[24:31], v134\n"
            "v_exp_f32 v35, v35\n"
            "v_exp_f32 v36, v36\n"
            "v_exp_f32 v37, v37\n"
            "s_waitcnt lgkmcnt(0)\n"
            "v_mfma_f32_32x32x4_2b_bf16 v[0:31], v[116:117], v[100:101], 0\n"
            "v_exp_f32 v38, v38\n"
            "v_exp_f32 v39, v39\n"
            "v_exp_f32 v40, v40\n"
            "v_exp_f32 v41, v41\n"
            "global_load_dwordx4 v[194:197], v141, s[64:65] offset:1024 sc1\n"
            "v_exp_f32 v42, v42\n"
            "v_exp_f32 v43, v43\n"
            "v_exp_f32 v44, v44\n"
            "v_exp_f32 v45, v45\n"
            "v_smfmac_f32_32x32x32_bf16 v[80:95], v[118:121], v[32:39], v134\n"
            "ds_read_b128 v[118:121], v140 offset:33280\n"
            "v_exp_f32 v46, v46\n"
            "v_exp_f32 v47, v47\n"
            "v_exp_f32 v48, v48\n"
            "v_exp_f32 v49, v49\n"
            "v_smfmac_f32_32x32x32_bf16 v[80:95], v[122:125], v[40:47], v134\n"
            "ds_read_b128 v[122:125], v140 offset:33984\n"
            "v_exp_f32 v50, v50\n"
            "v_exp_f32 v51, v51\n"
            "global_load_dwordx4 v[198:201], v141, s[64:65] offset:2048 sc1\n"
            "v_exp_f32 v52, v52\n"
            "v_mfma_f32_32x32x16_bf16 v[0:15], v[108:111], v[96:99], v[0:15]\n"
            "v_exp_f32 v53, v53\n"
            "v_exp_f32 v54, v54\n"
            "v_exp_f32 v55, v55\n"
            "v_exp_f32 v56, v56\n"
            "v_mfma_f32_32x32x16_bf16 v[16:31], v[112:115], v[96:99], v[16:31]\n"
            "v_exp_f32 v57, v57\n"
            "v_exp_f32 v58, v58\n"
            "v_exp_f32 v59, v59\n"
            "global_load_dwordx4 v[202:205], v141, s[64:65] offset:3072 sc1\n"
            "v_exp_f32 v60, v60\n"
            "v_smfmac_f32_32x32x32_bf16 v[80:95], v[126:129], v[48:55], v134\n"
            "ds_read_b128 v[126:129], v140 offset:35968\n"
            "v_exp_f32 v61, v61\n"
            "v_exp_f32 v62, v62\n"
            "v_exp_f32 v63, v63\n"
            "s_add_u32 s64, %[wlo], %[w3]\n"
            "s_addc_u32 s65, %[whi], 0\n"
            "v_exp_f32 v0, v0\n"
            "v_exp_f32 v1, v1\n"
            "global_load_dwordx4 v[206:209], v141, s[64:65] offset:0 sc1\n"
            "v_exp_f32 v2, v2\n"
            "v_smfmac_f32_32x32x32_bf16 v[80:95], v[130:133], v[56:63], v134\n"
            "ds_read_b128 v[130:133], v140 offset:36672\n"
            "v_exp_f32 v3, v3\n"
            "v_exp_f32 v4, v4\n"
            "v_exp_f32 v5, v5\n"
            "v_mfma_f32_32x32x4_2b_bf16 v[32:63], v[116:117], v[106:107], 0\n"
            "ds_read_b64 v[116:117], v139 offset:37632\n"
            "v_exp_f32 v6, v6\n"
            "v_exp_f32 v7, v7\n"
            "v_exp_f32 v8, v8\n"
            "v_exp_f32 v9, v9\n"
            "global_load_dwordx4 v[210:213], v141, s[64:65] offset:1024 sc1\n"
            "v_exp_f32 v10, v10\n"
            "v_exp_f32 v11, v11\n"
            "v_exp_f32 v12, v12\n"
            "v_exp_f32 v13, v13\n"
            "s_waitcnt lgkmcnt(1)\n"
            "v_smfmac_f32_32x32x32_bf16 v[64:79], v[118:121], v[0:7], v134\n"
            "v_exp_f32 v14, v14\n"
            "v_exp_f32 v15, v15\n"
            "v_exp_f32 v16, v16\n"
            "v_exp_f32 v17, v17\n"
            "v_smfmac_f32_32x32x32_bf16 v[64:79], v[122:125], v[8:15], v134\n"
            "v_exp_f32 v18, v18\n"
            "v_exp_f32 v19, v19\n"
            "global_load_dwordx4 v[214:217], v141, s[64:65] offset:2048 sc1\n"
            "v_exp_f32 v20, v20\n"
            "v_mfma_f32_32x32x16_bf16 v[32:47], v[108:111], v[102:105], v[32:47]\n"
            "ds_read_b128 v[108:111], v138 offset:37632\n"
            "v_exp_f32 v21, v21\n"
            "v_exp_f32 v22, v22\n"
            "v_exp_f32 v23, v23\n"
            "v_exp_f32 v24, v24\n"
            "v_mfma_f32_32x32x16_bf16 v[48:63], v[112:115], v[102:105], v[48:63]\n"
            "ds_read_b128 v[112:115], v138 offset:40320\n"
            "v_exp_f32 v25, v25\n"
            "v_exp_f32 v26, v26\n"
            "v_exp_f32 v27, v27\n"
            "global_load_dwordx4 v[218:221], v141, s[64:65] offset:3072 sc1\n"
            "v_exp_f32 v28, v28\n"
            "v_smfmac_f32_32x32x32_bf16 v[64:79], v[126:129], v[16:23], v134\n"
            "v_exp_f32 v29, v29\n"
            "v_exp_f32 v30, v30\n"
            "v_exp_f32 v31, v31\n"
            "s_add_u32 s64, s64, 4096\n"
            "s_addc_u32 s65, s65, 0\n"
            "v_exp_f32 v32, v32\n"
            "v_exp_f32 v33, v33\n"
            "global_load_dwordx4 v[222:225], v141, s[64:65] offset:0 sc1\n"
            "v_exp_f32 v34, v34\n"
            "v_smfmac_f32_32x32x32_bf16 v[64:79], v[130:133], v[24:31], v134\n"
            "v_exp_f32 v35, v35\n"
            "v_exp_f32 v36, v36\n"
            "v_exp_f32 v37, v37\n"
            "s_waitcnt lgkmcnt(0)\n"
            "v_mfma_f32_32x32x4_2b_bf16 v[0:31], v[116:117], v[100:101], 0\n"
            "v_exp_f32 v38, v38\n"
            "v_exp_f32 v39, v39\n"
            "v_exp_f32 v40, v40\n"
            "v_exp_f32 v41, v41\n"
            "global_load_dwordx4 v[226:229], v141, s[64:65] offset:1024 sc1\n"
            "v_exp_f32 v42, v42\n"
            "v_exp_f32 v43, v43\n"
            "v_exp_f32 v44, v44\n"
            "v_exp_f32 v45, v45\n"
            "v_smfmac_f32_32x32x32_bf16 v[80:95], v[118:121], v[32:39], v134\n"
            "ds_read_b128 v[118:121], v140 offset:38656\n"
            "v_exp_f32 v46, v46\n"
            "v_exp_f32 v47, v47\n"
            "v_exp_f32 v48, v48\n"
            "v_exp_f32 v49, v49\n"
            "v_smfmac_f32_32x32x32_bf16 v[80:95], v[122:125], v[40:47], v134\n"
            "ds_read_b128 v[122:125], v140 offset:39360\n"
            "v_exp_f32 v50, v50\n"
            "v_exp_f32 v51, v51\n"
            "global_load_dwordx4 v[230:233], v141, s[64:65] offset:2048 sc1\n"
            "v_exp_f32 v52, v52\n"
            "v_mfma_f32_32x32x16_bf16 v[0:15], v[108:111], v[96:99], v[0:15]\n"
            "v_exp_f32 v53, v53\n"
            "v_exp_f32 v54, v54\n"
            "v_exp_f32 v55, v55\n"
            "v_exp_f32 v56, v56\n"
            "v_mfma_f32_32x32x16_bf16 v[16:31], v[112:115], v[96:99], v[16:31]\n"
            "v_exp_f32 v57, v57\n"
            "v_exp_f32 v58, v58\n"
            "v_exp_f32 v59, v59\n"
            "global_load_dwordx4 v[234:237], v141, s[64:65] offset:3072 sc1\n"
            "v_exp_f32 v60, v60\n"
            "v_smfmac_f32_32x32x32_bf16 v[80:95], v[126:129], v[48:55], v134\n"
            "ds_read_b128 v[126:129], v140 offset:41344\n"
            "v_exp_f32 v61, v61\n"
            "v_exp_f32 v62, v62\n"
            "v_exp_f32 v63, v63\n"
            "v_exp_f32 v0, v0\n"
            "v_exp_f32 v1, v1\n"
            "v_exp_f32 v2, v2\n"
            "v_smfmac_f32_32x32x32_bf16 v[80:95], v[130:133], v[56:63], v134\n"
            "ds_read_b128 v[130:133], v140 offset:42048\n"
            "v_exp_f32 v3, v3\n"
            "v_exp_f32 v4, v4\n"
            "v_exp_f32 v5, v5\n"
            "v_mfma_f32_32x32x4_2b_bf16 v[32:63], v[116:117], v[106:107], 0\n"
            "v_exp_f32 v6, v6\n"
            "v_exp_f32 v7, v7\n"
            "v_exp_f32 v8, v8\n"
            "v_exp_f32 v9, v9\n"
            "v_exp_f32 v10, v10\n"
            "v_exp_f32 v11, v11\n"
            "v_exp_f32 v12, v12\n"
            "v_exp_f32 v13, v13\n"
            "s_waitcnt lgkmcnt(0)\n"
            "v_smfmac_f32_32x32x32_bf16 v[64:79], v[118:121], v[0:7], v134\n"
            "v_exp_f32 v14, v14\n"
            "v_exp_f32 v15, v15\n"
            "v_exp_f32 v16, v16\n"
            "v_exp_f32 v17, v17\n"
            "v_smfmac_f32_32x32x32_bf16 v[64:79], v[122:125], v[8:15], v134\n"
            "v_exp_f32 v18, v18\n"
            "v_exp_f32 v19, v19\n"
            "v_exp_f32 v20, v20\n"
            "v_mfma_f32_32x32x16_bf16 v[32:47], v[108:111], v[102:105], v[32:47]\n"
            "v_exp_f32 v21, v21\n"
            "v_exp_f32 v22, v22\n"
            "v_exp_f32 v23, v23\n"
            "v_exp_f32 v24, v24\n"
            "v_mfma_f32_32x32x16_bf16 v[48:63], v[112:115], v[102:105], v[48:63]\n"
            "v_exp_f32 v25, v25\n"
            "v_exp_f32 v26, v26\n"
            "v_exp_f32 v27, v27\n"
            "v_exp_f32 v28, v28\n"
            "v_smfmac_f32_32x32x32_bf16 v[64:79], v[126:129], v[16:23], v134\n"
            "v_exp_f32 v29, v29\n"
            "v_exp_f32 v30, v30\n"
            "v_exp_f32 v31, v31\n"
            "v_exp_f32 v32, v32\n"
            "v_exp_f32 v33, v33\n"
            "v_exp_f32 v34, v34\n"
            "v_smfmac_f32_32x32x32_bf16 v[64:79], v[130:133], v[24:31], v134\n"
            "v_exp_f32 v35, v35\n"
            "v_exp_f32 v36, v36\n"
            "v_exp_f32 v37, v37\n"
            "v_exp_f32 v38, v38\n"
            "v_exp_f32 v39, v39\n"
            "v_exp_f32 v40, v40\n"
            "v_exp_f32 v41, v41\n"
            "v_exp_f32 v42, v42\n"
            "v_exp_f32 v43, v43\n"
            "v_exp_f32 v44, v44\n"
            "v_exp_f32 v45, v45\n"
            "v_smfmac_f32_32x32x32_bf16 v[80:95], v[118:121], v[32:39], v134\n"
            "v_exp_f32 v46, v46\n"
            "v_exp_f32 v47, v47\n"
            "v_exp_f32 v48, v48\n"
            "v_exp_f32 v49, v49\n"
            "v_smfmac_f32_32x32x32_bf16 v[80:95], v[122:125], v[40:47], v134\n"
            "v_exp_f32 v50, v50\n"
            "v_exp_f32 v51, v51\n"
            "v_exp_f32 v52, v52\n"
            "v_exp_f32 v53, v53\n"
            "v_exp_f32 v54, v54\n"
            "v_exp_f32 v55, v55\n"
            "v_exp_f32 v56, v56\n"
            "v_exp_f32 v57, v57\n"
            "v_exp_f32 v58, v58\n"
            "v_exp_f32 v59, v59\n"
            "v_exp_f32 v60, v60\n"
            "v_smfmac_f32_32x32x32_bf16 v[80:95], v[126:129], v[48:55], v134\n"
            "v_exp_f32 v61, v61\n"
            "v_exp_f32 v62, v62\n"
            "v_exp_f32 v63, v63\n"
            "s_waitcnt vmcnt(0)\n"
            "s_nop 0\n"
            "v_smfmac_f32_32x32x32_bf16 v[80:95], v[130:133], v[56:63], v134\n"
            : "=&{v[64:79]}"(o0), "=&{v[80:95]}"(o1), "+{v[96:99]}"(qm0), "+{v[100:101]}"(qt0), "+{v[102:105]}"(qm1), "+{v[106:107]}"(qt1),
              "=&{v[142:173]}"(pk.a), "=&{v[174:205]}"(pk.b), "=&{v[206:237]}"(pk.c)
            : "{v138}"(kb), "{v139}"(tb), "{v140}"(vb), "{v141}"(c.voff), [wlo] "s"((unsigned)(uintptr_t)wpk), [whi] "s"((unsigned)((uintptr_t)wpk >> 32)), [w3] "s"(w < 3 ? 0 : 65536)
            : "v0", "v1", "v2", "v3", "v4", "v5", "v6", "v7", "v8", "v9", "v10", "v11", "v12", "v13", "v14", "v15", "v16", "v17", "v18", "v19", "v20", "v21", "v22", "v23", "v24", "v25", "v26", "v27", "v28", "v29", "v30", "v31", "v32", "v33", "v34", "v35", "v36", "v37", "v38", "v39", "v40", "v41", "v42", "v43", "v44", "v45", "v46", "v47", "v48", "v49", "v50", "v51", "v52", "v53", "v54", "v55", "v56", "v57", "v58", "v59", "v60", "v61", "v62", "v63", "v108", "v109", "v110", "v111", "v112", "v113", "v114", "v115", "v116", "v117", "v118", "v119", "v120", "v121", "v122", "v123", "v124", "v125", "v126", "v127", "v128", "v129", "v130", "v131", "v132", "v133", "v134", "s62", "s63", "s64", "s65", "vcc", "scc", "memory");
    }
    auto rowsum = [](const f32x16& o) { auto r = __builtin_amdgcn_permlane32_swap(__float_as_uint(o[8]), __float_as_uint(o[8]), false, false); return __uint_as_float(r[1]); };
#pragma unroll
    for (int u = 0; u < 2; ++u) {
        if (u == 1) { SBAR(); asm volatile("s_nop 0" : "+v"(o1)); SBAR(); }
        f32x16 o = u ? o1 : o0; float l = rowsum(o);
        float mag = 0.f;
#pragma unroll
        for (int r = 0; r < 12; r += 4) mag += (__builtin_fabsf(o[r]) + __builtin_fabsf(o[r + 1])) + (__builtin_fabsf(o[r + 2]) + __builtin_fabsf(o[r + 3]));
        if (__builtin_expect(__builtin_amdgcn_ballot_w64(!(l >= 0x1p-40f && l <= 0x1p64f && mag < INFINITY)) != 0, 0)) {
            const char* kvh = ws + WS_KV + layer * KV_BYTES + (size_t)c.m * 8 * KVCHUNK;
            const u32x4 qm = u ? qm1 : qm0; const u32x2 qt = u ? qt1 : qt0; const int idx = (int)0xDDDDDDDDu;
            const unsigned to = h * KVTILE + 2432 + t * 8, vo = h * 352 + (t < 21 ? t : 21) * 16;
            float mx = -INFINITY;
            for (int pass = 0; pass < 2; ++pass) {
                o = (f32x16){};
                for (int p = 0; p < 32; ++p) {
                    const char* pb = kvh + (size_t)p * KVPAIR; u32x4 k0, k1, v0, v1, v2, v3; u32x2 k2;
                    gload16<0>(k0, pb, c.voff); gload16<KVTILE>(k1, pb, c.voff); gload8<0>(k2, pb, to);
                    asm volatile("s_waitcnt vmcnt(0)" : "+v"(k0), "+v"(k1), "+v"(k2));
                    const f32x32 s = M2B(k2, qt, (f32x32){});
                    f32x16 s0 = MFMA32(k0, qm, half16<0>(s)), s1 = MFMA32(k1, qm, half16<16>(s));
                    if (pass == 0) {
#pragma unroll
                        for (int r = 0; r < 16; ++r) mx = __builtin_fmaxf(mx, __builtin_fmaxf(s0[r], s1[r]));
                    } else {
#pragma unroll
                        for (int r = 0; r < 16; ++r) { s0[r] = __builtin_amdgcn_exp2f(s0[r] - mx); s1[r] = __builtin_amdgcn_exp2f(s1[r] - mx); }
                        PIN(s0); PIN(s1);
                        gload16<1024>(v0, pb, vo); gload16<1728>(v1, pb, vo); gload16<3712>(v2, pb, vo); gload16<320>(v3, pb + 4096, vo);
                        asm volatile("s_waitcnt vmcnt(0)" : "+v"(v0), "+v"(v1), "+v"(v2), "+v"(v3));
                        o = PVMAC(o, v0, oct<0>(s0), idx); o = PVMAC(o, v1, oct<8>(s0), idx); o = PVMAC(o, v2, oct<0>(s1), idx); o = PVMAC(o, v3, oct<8>(s1), idx);
                    }
                }
                auto r = __builtin_amdgcn_permlane32_swap(__float_as_uint(mx), __float_as_uint(mx), false, false); mx = __builtin_fmaxf(__uint_as_float(r[0]), __uint_as_float(r[1]));
            }
            l = rowsum(o);
        }
        const float il = __builtin_amdgcn_rcpf(l);
        gstore16<0>(om + u * 32768, c.voff, (u32x4){cvtpk(o[0] * il, o[1] * il), cvtpk(o[2] * il, o[3] * il), cvtpk(o[4] * il, o[5] * il), cvtpk(o[6] * il, o[7] * il)});
        const u32x2 tl = {cvtpk(o[8] * il, o[9] * il), cvtpk(o[10] * il, o[11] * il)};
        if (h == 0) gstore8<0>(ot + u * 8192, t * 8, tl);
    }
}
__device__ __forceinline__ void glds3_dev(const char* sbase, unsigned voff, unsigned lds) {
    asm volatile("s_mov_b32 m0, %2\n\ts_nop 0\n\tglobal_load_lds_dwordx4 %0, %1 sc1\n\tglobal_load_lds_dwordx4 %0, %1 offset:1024 sc1\n\tglobal_load_lds_dwordx4 %0, %1 offset:2048 sc1" :: "v"(voff), "s"(sbase), "s"(lds) : "memory");
}
__device__ __forceinline__ void kv_flag(const Ctx& c, unsigned v) {
    if (drain_last(c) && c.lane < 8) __hip_atomic_store((GAS unsigned*)(c.ws + WS_FLK) + (c.m * 8 + c.lane) * 64 + c.g, v, __ATOMIC_RELAXED, __HIP_MEMORY_SCOPE_AGENT);
}
__device__ __forceinline__ void kv_fill(const Ctx& c_, int layer) {
    const Ctx c = fresh(c_);
    char* kd = c.ws + WS_KV + layer * KV_BYTES + (size_t)(c.m * 8 + c.g) * KVCHUNK + (c.w - 4) * KVPAIR;
    const u32x4 f = {~0u, ~0u, ~0u, ~0u};
    gstore16<0>(kd, c.voff, f); gstore16<1024>(kd, c.voff, f); gstore16<2048>(kd, c.voff, f); gstore16<3072>(kd, c.voff, f); gstore16<0>(kd + 4096, c.voff, f);
    if (c.lane < 16) gstore16<1024>(kd + 4096, c.voff, f);
}
__device__ __forceinline__ void fill_x(const Ctx& c_, int xbuf) {
    const Ctx c = fresh(c_);
    char* xd = c.ws + WS_X + (size_t)xbuf * X_BYTES + (size_t)((c.g * 8 + (c.m >> 3) * 2 + (c.w & 1)) * 40 + 5 * (c.m & 7) + (c.w & 2)) * 1024;
    const u32x4 f = {~0u, ~0u, ~0u, ~0u};
    gstore16<0>(xd, c.voff, f); gstore16<1024>(xd, c.voff, f); gstore16<2048>(xd, c.voff, f);
}
template <int PART> __device__ __forceinline__ void fill_oh(const Ctx& c_, int layer) {
    const Ctx c = fresh(c_);
    const int T = c.g * 8 + 2 * (c.w - 4);
    const u32x4 f = {~0u, ~0u, ~0u, ~0u};
    if constexpr (PART == 0) {
        char* om = c.ws + WS_OM + layer * OM_BYTES + (size_t)(T * 32 + c.m) * 1024; char* ot = c.ws + WS_OT + layer * OT_BYTES + (size_t)(T * 32 + c.m) * 256;
        gstore16<0>(om, c.voff, f); gstore16<0>(om + 32768, c.voff, f);
        if (c.lane < 32) gstore16<0>(ot, (c.lane >> 4) * 8192 + (c.lane & 15) * 16, f);
    } else {
        char* hd = c.ws + WS_H + layer * H_BYTES + (size_t)((T + PART - 1) * 96 + 3 * c.m) * 1024;
        gstore16<0>(hd, c.voff, f); gstore16<1024>(hd, c.voff, f); gstore16<2048>(hd, c.voff, f);
    }
}
struct Wsrc { const char *g, *u, *d; };
struct Retile { u32x2 r[3][8]; };
__device__ __forceinline__ void retile_load(const Ctx& c_, const Wsrc& w, Retile& t) {
    const Ctx c = fresh(c_);
#pragma unroll
    for (int k = 0; k < 3; ++k) {
        const int id = ((c.m >> 3) * 8 + c.g) * 8 + (c.w - 5) * 3 + (k == 2 && c.w == 7 ? 1 : k);
        const char* base; unsigned off, ld;
        if (id < 160) {
            const int b = id / 40, S = (id % 40) >> 1, q = c.lane >> 4, r = c.lane & 15;
            base = w.g + (id & 1) * (w.u - w.g); ld = FF * 2;
            off = (unsigned)phi(2 * S + (q >> 1), q & 1, 0) * ld + (((c.m & 7) + 8 * b) * 12 + (r < 12 ? r : 0)) * 8;
        } else {
            const int S = (id - 160) >> 1, h = c.lane >> 5, r = c.lane & 31;
            base = w.d; ld = DM * 2;
            off = (unsigned)phi(2 * S + (id & 1), h, 0) * ld + ((c.m & 7) * 20 + (r < 20 ? r : 0)) * 8;
        }
#pragma unroll
        for (int j = 0; j < 8; ++j) iload8<0>(t.r[k][j], base, off + (8 * (j >> 2) + (j & 3)) * ld);
    }
}
template <int k> __device__ __forceinline__ void retile_store(const Ctx& c_, Retile& t) {
    const Ctx c = fresh(c_);
    const unsigned sc = c.lds0 + L_RSC + (c.w - 4) * 4096;
    if (k < 2 || c.w < 7) {
        const int id = ((c.m >> 3) * 8 + c.g) * 8 + (c.w - 5) * 3 + k;
        u32x4 o[4];
#pragma unroll
        for (int j = 0; j < 8; ++j) PIN(t.r[k][j]);
        transpose8x4(t.r[k], o);
        if (id < 160) {
            const int b = id / 40, S = (id % 40) >> 1, up = id & 1, q = c.lane >> 4, r = c.lane & 15, sl = r >> 2, gi = r & 3;
            const int n = sl < 2 ? (gi >> 1) : 2, qq = sl < 2 ? 2 * sl + (gi & 1) : gi;
            const f32x4 cs = colsum8(o);
            if (r < 12) {
#pragma unroll
                for (int i = 0; i < 4; ++i) *(LAS u32x4*)(sc + n * 1024 + (q * 16 + 4 * qq + i) * 16) = o[i];
                *(LAS f32x4*)(sc + 3072 + q * 192 + (n * 16 + 4 * qq) * 4) = cs;
            }
            char* dst = c.ws + WS_WGU + ((c.m & 7) + 8 * b) * WG_BLOB;
            const u32x4 f0 = lds16(sc + c.voff), f1 = lds16(sc + 1024 + c.voff), f2 = lds16(sc + 2048 + c.voff);
            char* fd = dst + (S * 6 + 3 * up) * 1024;
            gstore16<0>(fd, c.voff, f0); gstore16<1024>(fd, c.voff, f1); gstore16<2048>(fd, c.voff, f2);
            if (c.lane < 12) {
                const LAS f32x4* cp = (const LAS f32x4*)(sc + 3072 + c.voff);
                gstore16<0>(dst + WG_CS + S * 384 + up * 192, c.voff, __builtin_bit_cast(u32x4, (cp[0] + cp[12]) + (cp[24] + cp[36])));
            }
        } else {
            const int S = (id - 160) >> 1, h = c.lane >> 5, r = c.lane & 31; int n, qq; proj_slot(r, n, qq);
            if (r < 20) {
#pragma unroll
                for (int i = 0; i < 4; ++i) *(LAS u32x4*)(sc + n * 512 + (h * 16 + 4 * qq + i) * 16) = o[i];
            }
            char* fd = c.ws + WS_WD + (c.m & 7) * WD_BLOB + S * 5120 + (id & 1) * 512;
            const unsigned fo = h * 1024 + r * 16;
            const u32x4 f0 = lds16(sc + c.voff), f1 = lds16(sc + 1024 + c.voff), f2 = lds16(sc + 2048 + r * 16);
            gstore16<0>(fd, fo, f0); gstore16<2048>(fd, fo, f1);
            if (h == 0) gstore16<0>(fd + 4096, fo, f2);
        }
    }
}
struct Stage { u32x4 a[31], b[25]; };
template <int I0, int I1, int N> __device__ __forceinline__ void stage_load(u32x4 (&r)[N], const char* src, unsigned voff) {
    sfor<I0, I1>([&](auto I_) { constexpr int i = I_; gload16<((i - I0) & 3) * 1024>(r[i], src + ((i - I0) >> 2) * 4096, voff); });
}
template <int I0, int I1, int N> __device__ __forceinline__ void stage_store(u32x4 (&r)[N], unsigned lds) {
    PIN(lds);
    sfor<I0, I1>([&](auto I_) { constexpr int i = I_; PIN(r[i]); *(LAS u32x4*)(lds + (i - I0) * 1024) = r[i]; });
}
__device__ __forceinline__ void help_attn(const Ctx& c_, int layer, Stage& sg, const Wsrc& wsrc) {
    const Ctx c = fresh(c_);
    const int hw = c.w - 4;
    char* kvh = c.ws + WS_KV + layer * KV_BYTES + (size_t)c.m * 8 * KVCHUNK;
    GAS unsigned* mine = (GAS unsigned*)(c.ws + WS_FLK) + (c.m * 8 + c.g) * 64;
    const unsigned k1 = (c.g + 1) & 7, full = 2 * layer + 1;
    Retile rt;
#pragma unroll
    for (int i = 0; i < 24; ++i) FRESH(rt.r[i >> 3][i & 7]);
    if (layer) poll(mine + k1, 2);
    wait_bar<0>();
    if (layer == 0 && hw) { __builtin_amdgcn_s_sleep(RT_SLEEP); retile_load(c, wsrc, rt); }
    __builtin_amdgcn_s_setprio(3);
#pragma unroll
    for (int b = 0; b < 2; ++b) {
        char* kd = kvh + (size_t)c.g * KVCHUNK + hw * KVPAIR + b * KVTILE; const unsigned pb = c.lds0 + hw * KVPAIR + b * KVTILE + c.voff;
        bar();
        const u32x4 b0 = lds16(pb), b1 = lds16(pb + 1024), b2 = lds16(pb + 2048);
        gstore16<0>(kd, c.voff, b0); gstore16<1024>(kd, c.voff, b1);
        if (c.lane < 40) gstore16<2048>(kd, c.voff, b2);
    }
    unsigned ready = 0;
    auto fetch = [&](int j) {
        const unsigned k = (c.g + j) & 7;
        for (unsigned spin = 0; !((ready >> k) & 1) && spin < (1u << 20); ++spin)
            ready = (unsigned)__ballot(__hip_atomic_load(mine + (c.lane & 7), __ATOMIC_RELAXED, __HIP_MEMORY_SCOPE_AGENT) >= full);
        const char* src = kvh + (size_t)k * KVCHUNK; const unsigned dst = c.lds0 + (j % 3) * KVCHUNK;
#pragma unroll
        for (int i = 0; i < 7; ++i) glds3_dev(src + i * 3072, c.voff, dst + i * 3072);
    };
    if (layer == 0) {
        kv_flag(c, full);
        if (hw == 0) { fetch(1); asm volatile("s_waitcnt vmcnt(0)" ::: "memory"); }
        else { retile_store<0>(c, rt); retile_store<1>(c, rt); retile_store<2>(c, rt); }
    } else {
        const char* src = kvh + (size_t)k1 * KVCHUNK + hw * KVPAIR; const unsigned vl = (c.lane & 15) * 16;
        u32x4 r[6]; bool take = false;
        __builtin_amdgcn_s_sleep(KV_SLEEP);
        for (unsigned spin = 0;; ++spin) {
            gload16<0>(r[0], src, c.voff); gload16<1024>(r[1], src, c.voff); gload16<2048>(r[2], src, c.voff); gload16<3072>(r[3], src, c.voff);
            gload16<0>(r[4], src + 4096, c.voff); gload16<1024>(r[5], src + 4096, vl);
            asm volatile("s_waitcnt vmcnt(0)" : "+v"(r[0]), "+v"(r[1]), "+v"(r[2]), "+v"(r[3]), "+v"(r[4]), "+v"(r[5]));
            if (spin == 0) { kv_flag(c, full); }
            unsigned mx = 0;
#pragma unroll
            for (int i = 0; i < 6; ++i) mx = max(max(mx, max(r[i][0], r[i][1])), max(r[i][2], r[i][3]));
            if (take || !__any(mx == ~0u)) break;
            take = spin >= (1u << 20) || __builtin_amdgcn_readfirstlane(__hip_atomic_load(mine + k1, __ATOMIC_RELAXED, __HIP_MEMORY_SCOPE_AGENT)) >= full;
        }
        const unsigned dst = c.lds0 + KVCHUNK + hw * KVPAIR;
#pragma unroll
        for (int i = 0; i < 5; ++i) *(LAS u32x4*)(dst + i * 1024 + c.voff) = r[i];
        if (c.lane < 16) *(LAS u32x4*)(dst + 5120 + c.voff) = r[5];
    }
    lds_bar();
    __builtin_amdgcn_s_setprio(0);
    const char* wo = c.ws + WS_WO + (c.m & 7) * WO_BLOB;
    const char* wg = c.ws + WS_WGU + c.m * WG_BLOB + (96 + hw * 6) * 1024;
    const char* wh = c.ws + WS_WD + (c.m & 7) * WD_BLOB + hw * 25 * 1024;
    const char* wd = c.ws + WS_WD + (c.m & 7) * WD_BLOB + 7 * 20480;
    sfor<2, 8>([&](auto J_) { constexpr int j = J_;
        if (hw == 0) fetch(j);
        else if (j < 6) glds<8>(wo + (j * 24 - 44 + (hw - 1) * 8) * 1024, c.voff, c.lds0 + L_WO + (j * 24 - 44 + (hw - 1) * 8) * 1024);
        if (layer == 0) {
            if constexpr (j == 3) { if (hw == 1 && c.lane < 32) __hip_atomic_store((GAS unsigned*)(c.ws + WS_FLW) + (((c.lane >> 3) * 8 + (c.m & 7)) * 8 + (c.lane & 7)) * 64 + (c.m >> 3) * 8 + c.g, 1u, __ATOMIC_RELAXED, __HIP_MEMORY_SCOPE_AGENT); }
            if constexpr (j == 4) poll((GAS unsigned*)(c.ws + WS_FLW) + (c.m * 8 + c.g) * 64 + (c.lane & 31), 1);
            if constexpr (j == 7) { if (hw == 1 && c.lane < 8) __hip_atomic_store((GAS unsigned*)(c.ws + WS_FLK) + (c.m * 8 + c.lane) * 64 + c.g, 2u, __ATOMIC_RELAXED, __HIP_MEMORY_SCOPE_AGENT); }
        }
        if constexpr (j == 6) { if (hw == 2 && c.lane < 32) __hip_atomic_store((GAS unsigned*)(c.ws + WS_FLG) + (c.lane * 8 + c.g) * 64 + c.m, layer + 1, __ATOMIC_RELAXED, __HIP_MEMORY_SCOPE_AGENT); }
        if constexpr (j == 7) { if (hw == 3) poll((GAS unsigned*)(c.ws + WS_FLG) + (c.m * 8 + c.g) * 64 + (c.lane & 31), layer + 1); }
        if constexpr (j == 4) { stage_load<25, 31>(sg.a, wg, c.voff); stage_load<0, 4>(sg.a, wh, c.voff); }
        if constexpr (j == 5) stage_load<4, 16>(sg.a, wh + 4096, c.voff);
        if constexpr (j == 6) { stage_load<16, 25>(sg.a, wh + 16384, c.voff); stage_load<0, 10>(sg.b, wd + hw * 10240, c.voff); }
        if constexpr (j == 7) { stage_load<10, 20>(sg.b, wd + 40960 + hw * 10240, c.voff); stage_load<20, 25>(sg.b, wd + 81920 + hw * 5120, c.voff); }
        if constexpr (j == 2) { fill_oh<0>(c, layer); fill_x(c, 2 * layer + 1); }
        if constexpr (j == 3) { fill_oh<1>(c, layer); if (layer == 0) fill_x(c, 2); }
        if constexpr (j == 4) fill_oh<2>(c, layer);
        if constexpr (j == 5) { if (layer == 0) kv_fill(c, 1); }
        if constexpr (j > 5) wait_bar<0>();
        else if (j == 2 || (j != 4 && layer == 0)) wait_bar<6>();
        else if (j == 5) wait_bar<0>();
        else wait_bar<3>();
    });
    if (hw == 1) glds<4>(wo, c.voff, c.lds0 + L_WO);
}

template <bool DOWN>
__device__ __forceinline__ void phase_proj(const Ctx& c_, int layer, uint16_t* out, Res& res, const Park& pk) {
    const Ctx c = fresh(c_);
    char* ws = c.ws; const int lane = c.lane, t = lane & 15, q = lane >> 4, cb = c.m & 7, kh = c.w >> 1;
    const int T = c.g * 8 + (c.m >> 3) * 2 + (c.w & 1), tt = 16 * kh + t, xin = 2 * layer + (DOWN ? 1 : 0);
    unsigned aoff = (q >> 1) * 1024 + (q & 1) * 512 + tt * 16, aoff1 = aoff ^ 256, xoff2 = (q & 1) * 512 + tt * 16 + (q >> 1) * 8, toff = q * 512 + tt * 8, toff1 = toff ^ 128;
    char* xd = ws + WS_X + (size_t)(xin + 1) * X_BYTES + (size_t)(T * 40 + 5 * cb) * 1024;
    f32x4 rs[5], acc[5], par[5]; float sc = 1.f;
    constexpr int NS = DOWN ? 24 : 10, WIN = DOWN ? DN_WIN : WO_WIN;
    const char* asrc = (DOWN ? ws + WS_H + layer * H_BYTES + (size_t)T * 96 * 1024 : ws + WS_OM + layer * OM_BYTES + (size_t)T * 32 * 1024) + kh * 4096;
    const char* tsrc = ws + WS_OT + layer * OT_BYTES + (size_t)T * 32 * 256 + kh * 4096;
    u32x4 a[NS][2]; u32x2 ta[8];
    auto issue = [&](auto I_, auto U_) { constexpr int i = I_, u = U_, o = (i & 1) * 2048;
        if constexpr (DOWN || i < 8) gload16<o>(a[i][u], asrc + (i >> 1) * 8192, u ? aoff1 : aoff);
        else { constexpr int k = (i & 1) * 4 + 2 * u; gload8<o>(ta[k], tsrc, u ? toff1 : toff); gload8<o + 256>(ta[k + 1], tsrc, u ? toff1 : toff); } };
    auto nops = [](int lo, int hi) { int n = 0; for (int s = lo; s <= hi; ++s) n += (DOWN || s < 8) ? 2 : 4; return n; };
    unsigned wa0 = c.lds0 + (DOWN ? 0 : L_WO) + kh * 10240 + c.voff, wa1 = wa0 + 61440, wa2 = wa0 + 122880;
    unsigned xa = c.lds0 + (DOWN ? 122880 : 0) + c.voff, xw = xa + c.w * 5120, xr = xa + (c.w ^ 2) * 5120;
    PIN(wa0); PIN(wa1); PIN(aoff); PIN(aoff1); PIN(xoff2); PIN(xw); PIN(xr); asm volatile("" : "+s"(asrc), "+s"(xd));
    if constexpr (DOWN) PIN(wa2); else { PIN(toff); PIN(toff1); asm volatile("" : "+s"(tsrc)); }
#pragma unroll
    for (int n = 0; n < 5; ++n) {
        const unsigned lo = n < 2 ? res.r0[2 * n] : (n < 4 ? res.r1[2 * n - 4] : res.r2[0]), hi = n < 2 ? res.r0[2 * n + 1] : (n < 4 ? res.r1[2 * n - 3] : res.r2[1]);
        rs[n] = (f32x4){bflo(lo), bfhi(lo), bflo(hi), bfhi(hi)}; PIN(rs[n]);
        if (!DOWN) acc[n] = rs[n];
    }
    open_phase<(DOWN ? OPEN_D : OPEN_O)>();
    sfor<0, WIN>([&](auto I_) { sfor<0, 2>([&](auto U_) { issue(I_, U_); }); });
    lds_bar();
    if constexpr (!DOWN) unpark(pk.a, c.lds0 + (24 + 8 * c.w) * 1024 + c.voff);
    u32x4 wf[2][5];
    auto wread = [&](auto I_, auto N_) { constexpr int i = I_, sl = (i >> 1) % 7, n = N_, o = (sl % 3) * 20480 + (i & 1) * 5120 + n * 1024;
        ldsr16<o>(wf[i & 1][n], sl < 3 ? wa0 : (sl < 6 ? wa1 : wa2)); };
    sfor<0, 5>([&](auto N_) { wread(std::integral_constant<int, 0>{}, N_); });
    unsigned mx; FRESH(mx); unsigned long long bad;
    auto word = [&](auto I_, auto J_) { constexpr int i = I_, j = J_; if constexpr (DOWN || i < 8) return a[i][j >> 1][(j & 1) * 2]; else return ta[(i & 1) * 4 + j][0]; };
    constexpr std::integral_constant<int, 0> I0; constexpr std::integral_constant<int, 1> I1; constexpr std::integral_constant<int, 2> I2; constexpr std::integral_constant<int, 3> I3;
    auto again = [&](auto I_) {
        for (unsigned spin = 0; spin < (1u << 18); ++spin) {
            constexpr int i = I_, k = (i & 1) * 4, E = i + WIN < NS ? i + WIN : NS;
            waitc<0, 15>();
            sfor<i, E>([&](auto R_) { issue(R_, I0); issue(R_, I1); });
            waitc<nops(i + 1, E - 1), 15>();
            if constexpr (DOWN || i < 8) pin2(a[i][0], a[i][1]); else { pin2(ta[k], ta[k + 1]); pin2(ta[k + 2], ta[k + 3]); }
            if (!stale(max3u(max(word(I_, I0), word(I_, I1)), word(I_, I2), word(I_, I3)))) break;
        } };
    waitc<nops(1, WIN - 1), 15>(); pin2(a[0][0], a[0][1]);
    if (RARE(stale(max3u(max(word(I0, I0), word(I0, I1)), word(I0, I2), word(I0, I3))))) again(I0);
    sfor<0, NS>([&](auto I_) { constexpr int i = I_, i1 = i < NS - 1 ? i + 1 : i;
        u32x4 (&w)[5] = wf[i & 1];
        waitc<63, (i < NS - 1 ? 2 : 0)>();
        if constexpr (!DOWN && i >= 8) { constexpr int k = (i & 1) * 4;
            pin2(ta[k], ta[k + 1]); pin2(ta[k + 2], ta[k + 3]);
            a[i][0] = (u32x4){ta[k][0], ta[k][1], ta[k + 1][0], ta[k + 1][1]}; a[i][1] = (u32x4){ta[k + 2][0], ta[k + 2][1], ta[k + 3][0], ta[k + 3][1]};
            asm volatile("s_nop 1" : "+v"(a[i][0]), "+v"(a[i][1]));
        }
        if constexpr (DOWN && (i == 3 || i == 9 || i == 17)) lds_bar();
        SBAR();
        if constexpr (i == NS - 1) {
            sfor<0, 5>([&](auto N_) { constexpr int n = N_; mfma16<false>(par[n], w[n], a[i][1]); });
            sfor<0, 4>([&](auto N_) { constexpr int n = N_; mfma16<false>(acc[n], w[n], a[i][0]); });
            ldsw16<0>(xw, par[0]); mfma16<false>(acc[4], w[4], a[i][0]);
            ldsw16<1024>(xw, par[1]); ldsw16<2048>(xw, par[2]); ldsw16<3072>(xw, par[3]); ldsw16<4096>(xw, par[4]);
        } else
        sfor<0, 10>([&](auto G_) { constexpr int g = G_, n = g >> 1;
            if constexpr (g & 1) mfma16<i == 0>(par[n], w[n], a[i][1]); else mfma16<i == 0 && DOWN>(acc[n], w[n], a[i][0]);
            SBAR();
            if constexpr (g < 5 && i < NS - 1) wread(std::integral_constant<int, i + 1>{}, std::integral_constant<int, g % 5>{});
            if constexpr (g == 5) {
                constexpr int last = i + WIN - 1 < NS - 1 ? i + WIN - 1 : NS - 1;
                waitc<nops(i1 + 1, last), 5>();
            }
            if constexpr ((g == 6 || g == 7) && i + WIN < NS) issue(std::integral_constant<int, (i + WIN < NS ? i + WIN : 0)>{}, std::integral_constant<int, g & 1>{});
            if constexpr (i < NS - 1) { constexpr std::integral_constant<int, i1> J;
                if constexpr (g == 6) chk_first(mx, word(J, I0), word(J, I1), word(J, I2));
                if constexpr (g == 7) chk_last(bad, mx, word(J, I3));
                if constexpr (g == 9) { if (RARE(bad != 0)) again(J); }
            }
            if constexpr (DOWN && i == 14 && g == 8) sc = *(const LAS float*)(c.lds0 + L_STAT + 1024 + ((c.m >> 3) * 64 + (c.w & 1) * 32 + tt) * 4);
            if constexpr (DOWN && i == 16 && g == 8) { sc *= sc; PIN(sc); }
            SBAR();
        });
    });
    lds_bar();
#pragma unroll
    for (int n = 0; n < 5; ++n) PIN(acc[n]);
#pragma unroll
    for (int n = 0; n < 5; ++n) par[n] = *(const LAS f32x4*)(xr + n * 1024);
#pragma unroll
    for (int n = 0; n < 5; ++n) acc[n] = DOWN ? (acc[n] + par[n]) * sc + rs[n] : acc[n] + par[n];
    u32x4 y0 = {cvtpk(acc[0][0], acc[0][1]), cvtpk(acc[0][2], acc[0][3]), cvtpk(acc[1][0], acc[1][1]), cvtpk(acc[1][2], acc[1][3])};
    u32x4 y1 = {cvtpk(acc[2][0], acc[2][1]), cvtpk(acc[2][2], acc[2][3]), cvtpk(acc[3][0], acc[3][1]), cvtpk(acc[3][2], acc[3][3])};
    u32x2 y2 = {cvtpk(acc[4][0], acc[4][1]), cvtpk(acc[4][2], acc[4][3])};
    PIN(y0); PIN(y1); PIN(y2);
    res.r0 = y0; res.r1 = y1; res.r2 = y2;
    if (DOWN && layer == 1) {
        uint16_t* op = out + (size_t)(32 * T + tt) * DM + 80 * cb;
        auto rows = [](const u32x4& y) {
            auto s0 = __builtin_amdgcn_permlane16_swap(y[0], y[2], false, false), s1 = __builtin_amdgcn_permlane16_swap(y[1], y[3], false, false);
            return (u32x4){s0[0], s1[0], s0[1], s1[1]}; };
        *(u32x4*)(op + 8 * q) = rows(y0); *(u32x4*)(op + 32 + 8 * q) = rows(y1); *(u32x2*)(op + 64 + 4 * q) = y2;
        return;
    }
    gstore16<0>(xd, aoff, y0); gstore16<2048>(xd, aoff, y1); gstore8<0>(xd + 4096, xoff2, y2);
    if (!DOWN) bar();
    SBAR();
    f32x2 v[10];
#pragma unroll
    for (int d = 0; d < 4; ++d) { v[d] = (f32x2){bflo(y0[d]), bfhi(y0[d])}; v[4 + d] = (f32x2){bflo(y1[d]), bfhi(y1[d])}; }
    v[8] = (f32x2){bflo(y2[0]), bfhi(y2[0])}; v[9] = (f32x2){bflo(y2[1]), bfhi(y2[1])};
    const f32x2 s2 = ((v[0] + v[1]) + (v[2] + v[3])) + ((v[4] + v[5]) + (v[6] + v[7])) + (v[8] + v[9]);
    const float sum = quad_add(s2[0] + s2[1]), mu = sum * (1.f / 80);
    f32x2 e0 = {}, e1 = {};
#pragma unroll
    for (int i = 0; i < 10; i += 2) { const f32x2 d0 = v[i] - mu, d1 = v[i + 1] - mu; e0 += d0 * d0; e1 += d1 * d1; }
    e0 += e1;
    const float m2 = quad_add(e0[0] + e0[1]);
    res.st = (u32x2){__float_as_uint(sum), __float_as_uint(m2)}; PIN(res.st);
}
__device__ __forceinline__ void help_mlp(const Ctx& c_, int layer, Stage& sg) {
    Ctx c = fresh(c_);
    const int hw = c.w - 4;
    wait_bar<0>();
    bar(); bar();
    auto chunk = [&](int ch) { const int pc = 20 * ch + (hw < 3 ? hw * 6 : 14);
        const char* src = c.ws + WS_WD + (c.m & 7) * WD_BLOB + pc * 1024;
        glds3_dev(src, c.voff, c.lds0 + pc * 1024); glds3_dev(src + 3072, c.voff, c.lds0 + (pc + 3) * 1024); };
    stage_store<25, 31>(sg.a, c.lds0 + (96 + hw * 6) * 1024 + c.voff);
    chunk(6);
    lds_bar();
    { Stats<0> st; stats_load(c, st, 2 * layer + 1); asm volatile("s_waitcnt vmcnt(0)" ::: "memory"); stats_finish(c, st); }
    if (c.w >= 6 && (7 - c.w) * 64 + c.lane < 96) {
        const int i = (7 - c.w) * 64 + c.lane;
        GAS float* p = (GAS float*)(c.ws + WS_WGU + WG_CS + c.m * WG_BLOB) + i;
        float v[20], sum = 0.f;
#pragma unroll
        for (int s = 0; s < 20; ++s) v[s] = __hip_atomic_load(p + s * 96, __ATOMIC_RELAXED, __HIP_MEMORY_SCOPE_AGENT);
#pragma unroll
        for (int s = 0; s < 20; ++s) sum += v[s];
        *(LAS float*)(c.lds0 + L_CS + i * 4) = sum;
    }
    lds_bar();
    bar();
    c = fresh(c);
    chunk(5);
    stage_store<0, 25>(sg.a, c.lds0 + hw * 25 * 1024 + c.voff);
    wait_bar<0>();
    const char* wq = c.ws + WS_WQKV + c.m * WQ_BLOB + hw * 20 * 1024;
#pragma unroll
    for (int i = 0; i < 20; ++i) FRESH(sg.a[i]);
    bar(); stage_store<0, 10>(sg.b, c.lds0 + hw * 10240 + c.voff);
    lds_bar(); stage_store<10, 20>(sg.b, c.lds0 + 40960 + hw * 10240 + c.voff); stage_store<20, 25>(sg.b, c.lds0 + 81920 + hw * 5120 + c.voff);
    lds_bar();
    if (layer == 0) stage_load<0, 20>(sg.a, wq, c.voff);
    bar();
    asm volatile("s_waitcnt vmcnt(0)" ::: "memory");
    if (layer == 0) {
        stage_store<0, 20>(sg.a, c.lds0 + L_WQ + hw * 20 * 1024 + c.voff);
        lds_bar();
    }
}

__device__ __forceinline__ void phase_gu(const Ctx& c_, int layer, const Res& res, const Park& pk) {
    const Ctx c = fresh(c_);
    char* ws = c.ws; const int t = c.lane & 15, q = c.lane >> 4, T = c.g * 8 + 2 * c.w;
    f32x4 acc[4][6], cs[6]; float mean[4];
    unsigned sa = c.lds0 + L_STAT + c.w * 256 + t * 4, ca = c.lds0 + L_CS + q * 16;
    unsigned ho1 = (q >> 1) * 1024 + (q & 1) * 512 + t * 16, ho2 = (q & 1) * 512 + t * 16 + (q >> 1) * 8;
    char* hd = ws + WS_H + layer * H_BYTES + (size_t)(T * 96 + 3 * c.m) * 1024; char* hd2 = hd + 96 * 1024;
    PIN(sa); PIN(ca); PIN(ho1); PIN(ho2); asm volatile("" : "+s"(hd), "+s"(hd2));
    gemm_tok64<6, GU_WIN, -1, GU_LD0>(c, ws + WS_X + (size_t)(2 * layer + 1) * X_BYTES + (size_t)T * 40 * 1024, c.lds0, acc, [&](int k) {
        if (k == 0) { put_stats(c, res, 2 * layer + 1); unpark(pk.b, c.lds0 + (56 + 8 * c.w) * 1024 + c.voff); }
        else unpark(pk.c, c.lds0 + (c.w < 3 ? 8 * c.w : 88) * 1024 + c.voff);
    }, [&](auto S_, auto G_) { constexpr int S = S_, g = G_, j = g >> 1;
        if constexpr (S == 10 && j == 0) bar();
        if constexpr (S == 10 && j >= 1 && j < 5) ldsrf<((j - 1) & 3) * 64>(mean[(j - 1) & 3], sa);
        sfor<0, 6>([&](auto K_) { constexpr int k = K_, i = (24 * S + g - (28 * k + 297)) / 2;
            if constexpr (i == -14) ldsrf<k * 64>(cs[k], ca);
            if constexpr (i >= 0 && i < 8) { constexpr int u = (i >> 1) & 3, hf = i & 1;
                fnma2<2 * hf>(acc[u][k], mean[u], cs[k][2 * hf], mean[u], cs[k][2 * hf + 1]); }
        });
    });
    bar();
#pragma unroll
    for (int u = 0; u < 4; ++u) {
        unsigned p[6];
#pragma unroll
        for (int n = 0; n < 3; ++n) {
            f32x4 gt = acc[u][n];
#pragma unroll
            for (int i = 0; i < 4; ++i) gt[i] = relu(gt[i]);
            const f32x4 hv = gt * acc[u][3 + n];
            p[2 * n] = cvtpk(hv[0], hv[1]); p[2 * n + 1] = cvtpk(hv[2], hv[3]);
        }
        if (u & 1) { gstore16<256>(u < 2 ? hd : hd2, ho1, (u32x4){p[0], p[1], p[2], p[3]}); gstore8<2304>(u < 2 ? hd : hd2, ho2, (u32x2){p[4], p[5]}); }
        else { gstore16<0>(u < 2 ? hd : hd2, ho1, (u32x4){p[0], p[1], p[2], p[3]}); gstore8<2048>(u < 2 ? hd : hd2, ho2, (u32x2){p[4], p[5]}); }
        SBAR();
    }
}

extern "C" __global__ __launch_bounds__(512, 2) void k_main(char* __restrict__ ws, uint16_t* __restrict__ out, const char* __restrict__ wg, const char* __restrict__ wu, const char* __restrict__ wd) {
    extern __shared__ __attribute__((aligned(16))) char smem[];
    Ctx c; c.ws = ws; c.lane = threadIdx.x & 63; c.w = __builtin_amdgcn_readfirstlane(threadIdx.x >> 6);
    c.g = (blockIdx.x & 1) * 4 + ((blockIdx.x >> 3) & 3); c.m = ((blockIdx.x >> 1) & 3) * 8 + (blockIdx.x >> 5); c.lds0 = (unsigned)(uintptr_t)smem; c.voff = c.lane * 16;
    if (threadIdx.x == 511) *(LAS unsigned*)(c.lds0 + L_CNT) = 0;
    if (c.w < 4) {
        Res res = {};
#pragma nounroll
        for (int layer = 0; layer < 2; ++layer) {
            Park pk;
            phase_qkv_attn(c, layer, res, pk);
            phase_proj<false>(c, layer, out, res, pk);
            phase_gu(c, layer, res, pk);
            phase_proj<true>(c, layer, out, res, pk);
        }
    } else {
#pragma nounroll
        for (int layer = 0; layer < 2; ++layer) {
            Stage sg;
            const int hw = c.w - 4, o0 = (c.m & 1) * 40960, o1 = 40960 - o0;
            const char* wq = ws + WS_WQKV + c.m * WQ_BLOB; const unsigned l = c.lds0 + L_WQ;
            Stats<4> st;
            if (layer == 0) glds<6>(wq + o0 + hw * 6144, c.voff, l + o0 + hw * 6144);
            stats_load(c, st, 2 * layer);
            if (layer == 0) {
                glds<4>(wq + o0 + 24576 + hw * 4096, c.voff, l + o0 + 24576 + hw * 4096); glds<10>(wq + o1 + hw * 10240, c.voff, l + o1 + hw * 10240);
                if (hw < 2) {
                    const char* code;
                    asm volatile("s_getpc_b64 s[20:21]\n\ts_add_u32 s20, s20, k_main@rel32@lo+4\n\ts_addc_u32 s21, s21, k_main@rel32@hi+12" : "={s[20:21]}"(code) :: "scc");
                    glds<1>(code + (blockIdx.x >> 3) * (CODE_LEN / 32) + hw * 1024, c.voff, c.lds0 + L_RSC + hw * 1024);
                }
                wait_bar<18>();
            }
            asm volatile("s_cmp_eq_u32 %0, 0\n\ts_cbranch_scc1 1f\n\ts_waitcnt vmcnt(0)\n1:\n\ts_waitcnt vmcnt(14)" :: "s"(layer) : "scc", "memory");
            stats_finish(c, st);
            wait_bar<10>();
            help_attn(c, layer, sg, Wsrc{wg, wu, wd});
            help_mlp(c, layer, sg);
        }
    }
}

extern "C" void kernel_launch(void* const* d_in, const int* in_sizes, int n_in, void* d_out, int out_size, void* d_ws, size_t ws_size, hipStream_t stream) {
    char* ws = (char*)d_ws;
    (void)hipFuncSetAttribute((const void*)k_main, hipFuncAttributeMaxDynamicSharedMemorySize, LDS_BYTES);
    k_prep<<<PREP_BLOCKS, 256, 0, stream>>>((const uint16_t*)d_in[0], (const uint16_t*)d_in[1], (const uint16_t*)d_in[2], ws);
    k_main<<<256, 512, LDS_BYTES, stream>>>(ws, (uint16_t*)d_out, (const char*)d_in[4], (const char*)d_in[5], (const char*)d_in[6]);
}
```
